```python
import torch

def _softmax(x, axis=-1):
    return torch.softmax(x, dim=axis)
def _gelu(x):
    return torch.nn.functional.gelu(x, approximate="tanh")
def _silu(x):
    return torch.nn.functional.silu(x)
def _sigmoid(x):
    return torch.sigmoid(x)
def _layernorm(x, axis=-1, eps=1e-5):
    m = x.mean(dim=axis, keepdim=True)
    v = x.var(dim=axis, keepdim=True, unbiased=False)
    return (x - m) / torch.sqrt(v + eps)
def _scatter_add(tgt, idx, src, axis=0):
    return tgt.clone().index_add_(axis, idx, src)
def _segment_sum(src, seg, n):
    out = torch.zeros((n,) + src.shape[1:], dtype=src.dtype)
    return out.index_add_(0, seg, src)
def _pad(x, pw):
    flat = tuple(v for pair in reversed(pw) for v in pair)
    return torch.nn.functional.pad(x, flat)

kernel_name = 'gated_mix__sliding_local_dilated__glu_gated_glu__prompt'

def setup_inputs():

    _w = (lambda a: (lambda t: t.to(torch.bfloat16) if t.is_floating_point() else t)(torch.as_tensor(a)))
    def _A_setup():
        B, S, D = 1, 2048, 512
        return {
            "x": _w((lambda *s: torch.randn(*s, dtype=torch.bfloat16) * 0.05)(B, S, D)),
            "wqkv": _w((lambda *s: torch.randn(*s, dtype=torch.bfloat16) * 0.8)(D, 3 * D)),
            "wo": _w((lambda *s: torch.randn(*s, dtype=torch.bfloat16) * 0.05)(D, D)),
        }
    _A_in = _A_setup()

    def _B_setup():
        B, S, D, F = 1, 2048, 512, 1792
        return {
            "x": _w((lambda *s: torch.randn(*s, dtype=torch.bfloat16) * 0.05)(B, S, D)),
            "w_gate": _w((lambda *s: torch.randn(*s, dtype=torch.bfloat16) * 3.2)(D, F)),
            "w_up": _w((lambda *s: torch.randn(*s, dtype=torch.bfloat16) * 0.1)(D, F)),
            "w_down": _w((lambda *s: torch.randn(*s, dtype=torch.bfloat16) * 0.1)(F, D)),
        }
    _B_in = _B_setup()

    return {
        "A_x": _A_in["x"],
        "A_wqkv": _A_in["wqkv"],
        "A_wo": _A_in["wo"],
        "B_x": _B_in["x"],
        "B_w_gate": _B_in["w_gate"],
        "B_w_up": _B_in["w_up"],
        "B_w_down": _B_in["w_down"],
        "gate_w": _w((lambda *s: torch.randn(*s, dtype=torch.bfloat16) * 0.05)(512) * 16.0),
    }

def reference(A_x, A_wqkv, A_wo, B_x, B_w_gate, B_w_up, B_w_down, gate_w):
    def _A_ref(x, wqkv, wo):
        B, S, D = x.shape
        H = 32; Dh = D // H
        W, dil = 8, 2
        qkv = (lambda x, *d: x.permute(*d))((x @ wqkv).reshape(B, S, 3, H, Dh), 2, 0, 3, 1, 4)
        q, k, v = qkv[0], qkv[1], qkv[2]
        scale = Dh ** -0.5
        pos = torch.arange(S)
        off = torch.arange(W) * dil
        idx = pos[:, None] - off[None, :]
        mask = idx >= 0
        idx_c = torch.where(mask, idx, 0).reshape(-1)
        kg = k[:, :, idx_c, :].reshape(B, H, S, W, Dh)
        vg = v[:, :, idx_c, :].reshape(B, H, S, W, Dh)
        logits = torch.einsum("bhsd,bhswd->bhsw", q, kg) * scale
        logits = torch.where(mask[None, None, :, :], logits, -1e9)
        attn = _softmax(logits, axis=-1)
        o = torch.einsum("bhsw,bhswd->bhsd", attn, vg)
        return (lambda x, *d: x.permute(*d))(o, 0, 2, 1, 3).reshape(B, S, D) @ wo

    def _B_ref(x, w_gate, w_up, w_down):
        g = torch.einsum("bsd,df->bsf", x, w_gate)
        u = torch.einsum("bsd,df->bsf", x, w_up)
        h = _sigmoid(g) * u
        return torch.einsum("bsf,fd->bsd", h, w_down)

    x = A_x
    g = _sigmoid((x * gate_w).sum(-1, keepdims=True))
    a = _A_ref(x, A_wqkv, A_wo)
    b = _B_ref(x, B_w_gate, B_w_up, B_w_down)
    return g * a + (1 - g) * b
```

```cpp
#include <hip/hip_runtime.h>
#include <hip/hip_bf16.h>
typedef __hip_bfloat16 bf16;
static __device__ __forceinline__ float b2f(bf16 x){ return __bfloat162float(x); }

__global__ void k_gemm(const bf16* A, const bf16* B, float* C, int M, int N, int K){
  __shared__ float As[16][17], Bs[16][17];
  int tx=threadIdx.x, ty=threadIdx.y; int row=blockIdx.y*16+ty, col=blockIdx.x*16+tx; float acc=0;
  for(int k0=0;k0<K;k0+=16){ As[ty][tx]=b2f(A[(size_t)row*K+k0+tx]); Bs[ty][tx]=b2f(B[(size_t)(k0+ty)*N+col]); __syncthreads();
    for(int k=0;k<16;k++) acc+=As[ty][k]*Bs[k][tx]; __syncthreads(); }
  C[(size_t)row*N+col]=acc;
}
__global__ void k_attn(const float* qkv, bf16* o){
  int idx=blockIdx.x*blockDim.x+threadIdx.x; int s=idx/32, h=idx%32; if(s>=2048) return;
  const float* q=qkv+(size_t)s*1536+h*16; float lg[8]; float m=-1e30f;
  for(int w=0;w<8;w++){ int t=s-2*w; if(t<0){lg[w]=-1e9f;} else { const float* k=qkv+(size_t)t*1536+512+h*16; float a=0; for(int d=0;d<16;d++) a+=q[d]*k[d]; lg[w]=a*0.25f;} m=fmaxf(m,lg[w]); }
  float sum=0; for(int w=0;w<8;w++){ lg[w]=expf(lg[w]-m); sum+=lg[w]; }
  for(int d=0;d<16;d++){ float a=0; for(int w=0;w<8;w++){ int t=s-2*w; if(t<0) t=0; a+=lg[w]*qkv[(size_t)t*1536+1024+h*16+d]; } o[(size_t)s*512+h*16+d]=__float2bfloat16(a/sum); }
}
__global__ void k_glu(const float* g, const float* u, bf16* h, int n){ int i=blockIdx.x*blockDim.x+threadIdx.x; if(i<n) h[i]=__float2bfloat16(u[i]/(1.f+expf(-g[i]))); }
__global__ void k_final(const bf16* x, const bf16* gw, const float* a, const float* b, bf16* out){
  int s=blockIdx.x; __shared__ float red[256]; float acc=0; for(int d=threadIdx.x;d<512;d+=256) acc+=b2f(x[s*512+d])*b2f(gw[d]); red[threadIdx.x]=acc; __syncthreads();
  for(int o=128;o>0;o>>=1){ if(threadIdx.x<o) red[threadIdx.x]+=red[threadIdx.x+o]; __syncthreads(); }
  float g=1.f/(1.f+expf(-red[0]));
  for(int d=threadIdx.x;d<512;d+=256) out[s*512+d]=__float2bfloat16(g*a[s*512+d]+(1.f-g)*b[s*512+d]);
}
extern "C" void kernel_launch(void* const* d_in, const int* in_sizes, int n_in, void* d_out, int out_size, void* d_ws, size_t ws_size, hipStream_t stream) {
  const bf16* Ax=(const bf16*)d_in[0]; const bf16* wqkv=(const bf16*)d_in[1]; const bf16* wo=(const bf16*)d_in[2]; const bf16* Bx=(const bf16*)d_in[3];
  const bf16* wg=(const bf16*)d_in[4]; const bf16* wu=(const bf16*)d_in[5]; const bf16* wd=(const bf16*)d_in[6]; const bf16* gw=(const bf16*)d_in[7];
  char* ws=(char*)d_ws; float* qkv=(float*)ws; float* g=(float*)(ws+(16<<20)); float* u=(float*)(ws+(32<<20)); bf16* o=(bf16*)(ws+(48<<20)); bf16* h=(bf16*)(ws+(56<<20)); float* a=(float*)(ws+(64<<20)); float* b=(float*)(ws+(72<<20));
  dim3 blk(16,16);
  k_gemm<<<dim3(1536/16,128),blk,0,stream>>>(Ax,wqkv,qkv,2048,1536,512);
  k_attn<<<2048*32/256,256,0,stream>>>(qkv,o);
  k_gemm<<<dim3(512/16,128),blk,0,stream>>>(o,wo,a,2048,512,512);
  k_gemm<<<dim3(1792/16,128),blk,0,stream>>>(Ax,wg,g,2048,1792,512);
  k_gemm<<<dim3(1792/16,128),blk,0,stream>>>(Ax,wu,u,2048,1792,512);
  k_glu<<<2048*1792/256,256,0,stream>>>(g,u,h,2048*1792);
  k_gemm<<<dim3(512/16,128),blk,0,stream>>>(h,wd,b,2048,512,1792);
  k_final<<<2048,256,0,stream>>>(Ax,gw,a,b,(bf16*)d_out);
}
```

```cpp
#include <hip/hip_runtime.h>
#include <hip/hip_bf16.h>
typedef __hip_bfloat16 bf16;
static __device__ __forceinline__ float b2f(bf16 x){ return __bfloat162float(x); }

__global__ void k_gemm(const bf16* A, const bf16* B, float* C, int M, int N, int K){
  __shared__ float As[16][17], Bs[16][17];
  int tx=threadIdx.x, ty=threadIdx.y; int row=blockIdx.y*16+ty, col=blockIdx.x*16+tx; float acc=0;
  for(int k0=0;k0<K;k0+=16){ As[ty][tx]=b2f(A[(size_t)row*K+k0+tx]); Bs[ty][tx]=b2f(B[(size_t)(k0+ty)*N+col]); __syncthreads();
    for(int k=0;k<16;k++) acc+=As[ty][k]*Bs[k][tx]; __syncthreads(); }
  C[(size_t)row*N+col]=acc;
}
__global__ void k_attn(const float* qkv, bf16* o){
  int idx=blockIdx.x*blockDim.x+threadIdx.x; int s=idx/32, h=idx%32; if(s>=2048) return;
  const float* q=qkv+(size_t)s*1536+h*16; float lg[8]; float m=-1e30f;
  for(int w=0;w<8;w++){ int t=s-2*w; if(t<0){lg[w]=-1e9f;} else { const float* k=qkv+(size_t)t*1536+512+h*16; float a=0; for(int d=0;d<16;d++) a+=q[d]*k[d]; lg[w]=a*0.25f;} m=fmaxf(m,lg[w]); }
  float sum=0; for(int w=0;w<8;w++){ lg[w]=expf(lg[w]-m); sum+=lg[w]; }
  for(int d=0;d<16;d++){ float a=0; for(int w=0;w<8;w++){ int t=s-2*w; if(t<0) t=0; a+=lg[w]*qkv[(size_t)t*1536+1024+h*16+d]; } o[(size_t)s*512+h*16+d]=__float2bfloat16(a/sum); }
}
__global__ void k_glu(const float* g, const float* u, bf16* h, int n){ int i=blockIdx.x*blockDim.x+threadIdx.x; if(i<n) h[i]=__float2bfloat16(u[i]/(1.f+expf(-g[i]))); }
__global__ void k_final(const bf16* x, const bf16* gw, const float* a, const float* b, bf16* out){
  int s=blockIdx.x; __shared__ float red[256]; float acc=0; for(int d=threadIdx.x;d<512;d+=256) acc+=b2f(x[s*512+d])*b2f(gw[d]); red[threadIdx.x]=acc; __syncthreads();
  for(int o=128;o>0;o>>=1){ if(threadIdx.x<o) red[threadIdx.x]+=red[threadIdx.x+o]; __syncthreads(); }
  float g=1.f/(1.f+expf(-red[0]));
  for(int d=threadIdx.x;d<512;d+=256) out[s*512+d]=__float2bfloat16(g*a[s*512+d]+(1.f-g)*b[s*512+d]);
}

__global__ void k_probe(const unsigned* p, int n, int stride_words, unsigned* sink){
  unsigned idx=0, acc=0;
  for(int i=0;i<n;i++){ unsigned v=p[idx]; asm volatile("s_waitcnt vmcnt(0)\n v_and_b32 %0, 0, %0" : "+v"(v)); acc+=v; idx=idx+stride_words+v; }
  if(acc==12345u) sink[0]=acc;
}
extern "C" void kernel_launch(void* const* d_in, const int* in_sizes, int n_in, void* d_out, int out_size, void* d_ws, size_t ws_size, hipStream_t stream) {
  const bf16* Ax=(const bf16*)d_in[0]; const bf16* wqkv=(const bf16*)d_in[1]; const bf16* wo=(const bf16*)d_in[2]; const bf16* Bx=(const bf16*)d_in[3];
  const bf16* wg=(const bf16*)d_in[4]; const bf16* wu=(const bf16*)d_in[5]; const bf16* wd=(const bf16*)d_in[6]; const bf16* gw=(const bf16*)d_in[7];
  char* ws=(char*)d_ws; float* qkv=(float*)ws; float* g=(float*)(ws+(16<<20)); float* u=(float*)(ws+(32<<20)); bf16* o=(bf16*)(ws+(48<<20)); bf16* h=(bf16*)(ws+(56<<20)); float* a=(float*)(ws+(64<<20)); float* b=(float*)(ws+(72<<20));
  dim3 blk(16,16);
  k_probe<<<1,1,0,stream>>>((const unsigned*)(d_in[0]), 1, 512, (unsigned*)(ws+(100<<20)));
  k_gemm<<<dim3(1536/16,128),blk,0,stream>>>(Ax,wqkv,qkv,2048,1536,512);
  k_attn<<<2048*32/256,256,0,stream>>>(qkv,o);
  k_gemm<<<dim3(512/16,128),blk,0,stream>>>(o,wo,a,2048,512,512);
  k_gemm<<<dim3(1792/16,128),blk,0,stream>>>(Ax,wg,g,2048,1792,512);
  k_gemm<<<dim3(1792/16,128),blk,0,stream>>>(Ax,wu,u,2048,1792,512);
  k_glu<<<2048*1792/256,256,0,stream>>>(g,u,h,2048*1792);
  k_gemm<<<dim3(512/16,128),blk,0,stream>>>(h,wd,b,2048,512,1792);
  k_final<<<2048,256,0,stream>>>(Ax,gw,a,b,(bf16*)d_out);
}
```

```cpp
#include <hip/hip_runtime.h>
#include <hip/hip_bf16.h>

#define LDSP __attribute__((address_space(3)))
#define DI __device__ __forceinline__
typedef short s16x4 __attribute__((ext_vector_type(4)));
typedef __bf16 bf16x8 __attribute__((ext_vector_type(8)));
typedef __bf16 bf16x2 __attribute__((ext_vector_type(2)));
typedef float f32x2 __attribute__((ext_vector_type(2)));
typedef float f32x4 __attribute__((ext_vector_type(4)));
typedef float f32x16 __attribute__((ext_vector_type(16)));
typedef unsigned u32x2 __attribute__((ext_vector_type(2)));
typedef unsigned u32x4 __attribute__((ext_vector_type(4)));
typedef int i32x4 __attribute__((ext_vector_type(4)));

namespace {

constexpr int SEQ = 2048, DM = 512, FF = 1792;
constexpr int WSLAB = 32 * 640;
constexpr int XSLAB = 144 * 128;
constexpr int NWS = 5, NXS = 3;
constexpr int LDS_X = NWS * WSLAB;
constexpr int LDS_S1 = LDS_X + NXS * XSLAB;
constexpr int LDS_TOTAL = 163840;
constexpr int NKT = 144;
constexpr size_t ACT_BYTES = (size_t)SEQ * NKT * 16 * 2;
constexpr size_t WS_ACT = 0, WS_G = ACT_BYTES;

DI void dma16(const void* g, unsigned lds) {
    asm volatile("s_mov_b32 m0, %1\n\ts_nop 0\n\tglobal_load_lds_dwordx4 %0, off" ::"v"(g), "s"(lds) : "memory");
}
DI void dma16b(i32x4 srd, unsigned voff, unsigned soff, unsigned lds) {
    asm volatile("s_mov_b32 m0, %3\n\ts_nop 0\n\tbuffer_load_dwordx4 %0, %1, %2 offen lds" ::"v"(voff), "s"(srd), "s"(soff), "s"(lds) : "memory");
}
DI void dma16b_half(i32x4 srd, unsigned voff, unsigned soff, unsigned lds) {
    asm volatile("s_mov_b32 exec_hi, 0\n\ts_mov_b32 m0, %3\n\ts_nop 0\n\tbuffer_load_dwordx4 %0, %1, %2 offen lds\n\ts_mov_b32 exec_hi, -1"
                 ::"v"(voff), "s"(srd), "s"(soff), "s"(lds) : "memory");
}
DI void dma4b(i32x4 srd, unsigned voff, unsigned soff, unsigned lds) {
    asm volatile("s_mov_b32 m0, %3\n\ts_nop 0\n\tbuffer_load_dword %0, %1, %2 offen lds" ::"v"(voff), "s"(srd), "s"(soff), "s"(lds) : "memory");
}
template <int N> DI void wait_vm() { asm volatile("s_waitcnt vmcnt(%0)" ::"n"(N) : "memory"); }
DI void wait_lgkm0() { asm volatile("s_waitcnt lgkmcnt(0)" ::: "memory"); }
DI void barrier() { asm volatile("" ::: "memory"); __builtin_amdgcn_s_barrier(); asm volatile("" ::: "memory"); }
DI i32x4 make_srd(const void* p, unsigned bytes) {
    const unsigned long long a = (unsigned long long)p;
    i32x4 r; r[0] = (int)(unsigned)a; r[1] = (int)((unsigned)(a >> 32) & 0xffffu); r[2] = (int)bytes; r[3] = 0x00020000; return r;
}
DI unsigned pk2(float a, float b) { f32x2 v = {a, b}; return __builtin_bit_cast(unsigned, __builtin_convertvector(v, bf16x2)); }
DI u32x2 pk4(f32x4 v) { u32x2 r; r[0] = pk2(v[0], v[1]); r[1] = pk2(v[2], v[3]); return r; }
DI bf16x8 cat8(u32x2 a, u32x2 b) { u32x4 r = {a[0], a[1], b[0], b[1]}; return __builtin_bit_cast(bf16x8, r); }
DI f32x4 mfma32(bf16x8 a, bf16x8 b, f32x4 c) { return __builtin_amdgcn_mfma_f32_16x16x32_bf16(a, b, c, 0, 0, 0); }
DI f32x4 mfma16(u32x2 a, u32x2 b, f32x4 c) {
    return __builtin_amdgcn_mfma_f32_16x16x16bf16_1k(__builtin_bit_cast(s16x4, a), __builtin_bit_cast(s16x4, b), c, 0, 0, 0);
}
DI bf16x8 lds_row(LDSP char* base, unsigned off) { return *(LDSP bf16x8*)(base + off); }
DI bf16x8 lds_tr(LDSP char* base, unsigned off, unsigned step) {
    s16x4 a = __builtin_amdgcn_ds_read_tr16_b64_v4i16((LDSP s16x4*)(base + off));
    s16x4 b = __builtin_amdgcn_ds_read_tr16_b64_v4i16((LDSP s16x4*)(base + off + step));
    return __builtin_bit_cast(bf16x8, __builtin_shufflevector(a, b, 0, 1, 2, 3, 4, 5, 6, 7));
}
DI float max8(f32x4 a, f32x4 b) {
    float m;
    asm volatile("v_max3_f32 %0, %1, %2, %3\n\tv_max3_f32 %0, %0, %4, %5\n\tv_max3_f32 %0, %0, %6, %7\n\tv_max_f32 %0, %0, %8"
                 : "=&v"(m) : "v"(a[0]), "v"(a[1]), "v"(a[2]), "v"(a[3]), "v"(b[0]), "v"(b[1]), "v"(b[2]), "v"(b[3]));
    return m;
}
DI void colmax4(float& m0, float& m1, float& m2, float& m3) {
    float t0, t1;
    asm volatile("s_nop 1\n\t"
                 "v_permlane16_swap_b32 %0, %1\n\tv_permlane16_swap_b32 %2, %3\n\t"
                 "v_max_f32 %0, %0, %1\n\tv_max_f32 %2, %2, %3\n\ts_nop 1\n\t"
                 "v_permlane32_swap_b32 %0, %2\n\t"
                 "v_max_f32 %0, %0, %2\n\t"
                 "v_mov_b32 %2, %0\n\ts_nop 1\n\t"
                 "v_permlane32_swap_b32 %0, %2\n\t"
                 "v_mov_b32 %1, %0\n\tv_mov_b32 %3, %2\n\ts_nop 1\n\t"
                 "v_permlane16_swap_b32 %0, %1\n\tv_permlane16_swap_b32 %2, %3\n\ts_nop 1"
                 : "+v"(m0), "+v"(m1), "+v"(m2), "+v"(m3));
    (void)t0; (void)t1;
}
DI float mx(float a, float b) { return __builtin_amdgcn_fmed3f(a, b, __builtin_inff()); }
DI float xmax16(float m) { u32x2 r = __builtin_amdgcn_permlane16_swap(__float_as_uint(m), __float_as_uint(m), false, false); return mx(__uint_as_float(r[0]), __uint_as_float(r[1])); }
DI float xmax32(float m) { u32x2 r = __builtin_amdgcn_permlane32_swap(__float_as_uint(m), __float_as_uint(m), false, false); return mx(__uint_as_float(r[0]), __uint_as_float(r[1])); }
DI u32x4 frag32(u32x2 e, u32x2 o) {
    u32x2 a = __builtin_amdgcn_permlane16_swap(e[0], o[0], false, false);
    u32x2 b = __builtin_amdgcn_permlane16_swap(e[1], o[1], false, false);
    u32x4 r = {a[0], b[0], a[1], b[1]};
    return r;
}
DI float sigmoidf_(float g) { return __builtin_amdgcn_rcpf(1.f + __builtin_amdgcn_exp2f(g * -1.4426950408889634f)); }
DI f32x4 glu4(f32x4 g, f32x4 u) { f32x4 h; for (int j = 0; j < 4; ++j) h[j] = u[j] * sigmoidf_(g[j]); return h; }

struct Params {
    const char* x; const char* wqkv; const char* wo; const char* wg; const char* wu; const char* wd; const char* gw;
    char* out; char* ws;
};

#define STAMP(c, i) do {} while (0)
#define FENCE() __builtin_amdgcn_sched_barrier(0)

template <bool SC1> DI void st16(char* base, unsigned off, u32x4 v) {
    if constexpr (SC1) {
        auto r = __builtin_amdgcn_make_buffer_rsrc((void*)base, 0, 0x7fffffff, 0x00020000);
        __builtin_amdgcn_raw_buffer_store_b128(v, r, off, 0, 16);
    } else {
        *(u32x4*)(base + off) = v;
    }
}

constexpr int NSTEP = 16;
constexpr int NPHASE = 2 + NSTEP, MAXOPS = 9;
struct Sched { int n[NPHASE]; int op[NPHASE][MAXOPS]; };
constexpr int opw(int slab, int part) { return 4 * slab + part; }
constexpr int opx(int slab, int part) { return 64 + 4 * slab + part; }
constexpr Sched make_sched(bool X) {
    Sched s{};
    auto addw = [&](int ph, int slab) { for (int p = 0; p < 3; ++p) s.op[ph][s.n[ph]++] = opw(slab, p); };
    auto addx = [&](int ph, int slab, int p0, int p1) { for (int p = p0; p <= p1; ++p) if (p < 2 || X) s.op[ph][s.n[ph]++] = opx(slab, p); };
    addw(0, 0); addx(0, 0, 0, 2);
    addw(1, 1); addx(1, 1, 0, 2);
    addw(2, 2); addw(2, 3);
    addw(3, 4); addx(3, 2, 0, 2);
    addw(4, 5); addw(4, 6);
    addw(5, 7); addx(5, 3, 0, 2);
    addw(6, 8); addw(6, 9);
    addw(7, 10); addx(7, 4, 0, 2);
    addw(8, 11); addx(8, 5, 0, 2);
    for (int T = 7; T <= 10; ++T) {
        addw(2 + T, T + 5);
        if (T & 1) addx(2 + T, (T - 1) / 2 + 3, 0, 0); else addx(2 + T, T / 2 + 2, 1, 2);
    }
    return s;
}
template <bool X> struct SchedOf { static constexpr Sched v = make_sched(X); };
constexpr int in_flight_ok(bool X, int s, int ph_end) {
    const Sched sc = make_sched(X);
    int n = 0, need = 0;
    for (int ph = 0; ph < ph_end; ++ph)
        for (int i = 0; i < sc.n[ph]; ++i) {
            const int o = sc.op[ph][i], slab = (o & 63) >> 2;
            ++n;
            if (o < 64 ? slab == s : slab == (s >> 1)) need = n;
        }
    return n - need;
}

struct S1Ctx {
    LDSP char* lds;
    i32x4 xsrd, srd[3];
    unsigned wvo[3], wso[3], wstr[3], wdst[3];
    unsigned xvo, hvo, xdst, hdst;
};
template <int N> DI void issue_w(const S1Ctx& c, int slab) {
    const unsigned so = c.wso[N] + (unsigned)slab * c.wstr[N], dst = (unsigned)(slab % NWS) * WSLAB + c.wdst[N];
    if constexpr (N == 2) dma16b_half(c.srd[N], c.wvo[N], so, dst);
    else dma16b(c.srd[N], c.wvo[N], so, dst);
}
template <int PART> DI void issue_x(const S1Ctx& c, int xs) {
    const unsigned slot = (unsigned)(xs % NXS) * XSLAB;
    if constexpr (PART == 2) dma4b(c.xsrd, c.hvo, (unsigned)xs * 128u, slot + c.hdst);
    else dma16b(c.xsrd, c.xvo, (unsigned)xs * 128u + PART * 16384u, slot + c.xdst + PART * 1024u);
}
template <bool X, int PH, int J> DI void s1_op(const S1Ctx& c) {
    if constexpr (J < SchedOf<X>::v.n[PH]) {
        constexpr int o = SchedOf<X>::v.op[PH][J], slab = (o & 63) >> 2, part = o & 3;
        if constexpr (o < 64) issue_w<part>(c, slab); else issue_x<part>(c, slab);
    }
}
template <bool X, int PH, int J> DI void s1_ops_all(const S1Ctx& c) {
    if constexpr (J < SchedOf<X>::v.n[PH]) { s1_op<X, PH, J>(c); s1_ops_all<X, PH, J + 1>(c); }
}
template <bool X, int S> DI void s1_top(S1Ctx& c) {
    if constexpr (S + 1 < NSTEP) wait_vm<in_flight_ok(X, S + 1, 2 + S)>();
    wait_lgkm0();
    barrier();
    STAMP(c, 2 + S);
    FENCE();
}

struct FragA { bf16x8 x[5], w[5]; };
struct FragB { bf16x8 x[4], w[6]; };
struct AddrA { unsigned w[2], x[2], h[2]; };
struct AddrB { unsigned w[2][2], wh[2], x[4]; };
struct AccA { f32x4 q[4], k[5], v[5], g[4], u[4]; };
struct AccB { f32x4 g0[4], u0[4], g1[4], u1[4], g2[2], u2[2]; };

DI s16x4 lds_tr1(LDSP char* base, unsigned off) { return __builtin_amdgcn_ds_read_tr16_b64_v4i16((LDSP s16x4*)(base + off)); }
DI void set_half(bf16x8& d, s16x4 v, int hi) {
    u32x4 t = __builtin_bit_cast(u32x4, d); const u32x2 s = __builtin_bit_cast(u32x2, v);
    t[2 * hi] = s[0]; t[2 * hi + 1] = s[1]; d = __builtin_bit_cast(bf16x8, t);
}
template <int S, int I> DI void rdA(FragA& f, LDSP char* lds, const AddrA& a) {
    constexpr int hi = (S % NWS) >= 2 ? 1 : 0;
    constexpr unsigned ws = (unsigned)(S % NWS - 2 * hi) * WSLAB, xs = (unsigned)((S >> 1) % NXS) * XSLAB;
    if constexpr (I < 4) f.x[I] = lds_row(lds, a.x[S & 1] + xs + I * 2048);
    else if constexpr (I == 4) f.x[4] = lds_row(lds, a.h[S & 1] + xs);
    else if constexpr (I < 15) { constexpr int n = (I - 5) >> 1, t = (I - 5) & 1; set_half(f.w[n], lds_tr1(lds, a.w[hi] + ws + n * 4096 + t * 512), t); }
}
template <int S, int I> DI void rdB(FragB& f, LDSP char* lds, const AddrB& a) {
    constexpr int hi = (S % NWS) >= 2 ? 1 : 0;
    constexpr unsigned ws = (unsigned)(S % NWS - 2 * hi) * WSLAB, xs = (unsigned)((S >> 1) % NXS) * XSLAB;
    if constexpr (I < 4) f.x[I] = lds_row(lds, a.x[2 * (I >> 1) + (S & 1)] + xs + I * 2048);
    else if constexpr (I < 12) { constexpr int n = (I - 4) >> 1, t = (I - 4) & 1; set_half(f.w[n], lds_tr1(lds, a.w[hi][n >> 1] + ws + (n & 1) * 8192 + t * 512), t); }
    else if constexpr (I < 16) { constexpr int n = (I - 12) >> 1, t = (I - 12) & 1; set_half(f.w[4 + n], lds_tr1(lds, a.wh[hi] + ws + n * 2048 + t * 256), t); }
}
template <int M> DI void mmA(AccA& a, const FragA& f) {
    if constexpr (M < 5) a.k[M] = mfma32(f.w[1], f.x[M], a.k[M]);
    else if constexpr (M < 10) a.v[M - 5] = mfma32(f.x[M - 5], f.w[2], a.v[M - 5]);
    else if constexpr (M < 14) a.q[M - 10] = mfma32(f.w[0], f.x[M - 10], a.q[M - 10]);
    else if constexpr (M < 18) a.g[M - 14] = mfma32(f.w[3], f.x[M - 14], a.g[M - 14]);
    else a.u[M - 18] = mfma32(f.w[4], f.x[M - 18], a.u[M - 18]);
}
template <int M> DI void mmB(AccB& a, const FragB& f) {
    if constexpr (M < 4) a.g0[M] = mfma32(f.w[0], f.x[M], a.g0[M]);
    else if constexpr (M < 8) a.u0[M - 4] = mfma32(f.w[1], f.x[M - 4], a.u0[M - 4]);
    else if constexpr (M < 12) a.g1[M - 8] = mfma32(f.w[2], f.x[M - 8], a.g1[M - 8]);
    else if constexpr (M < 16) a.u1[M - 12] = mfma32(f.w[3], f.x[M - 12], a.u1[M - 12]);
    else if constexpr (M < 18) a.g2[M - 16] = mfma32(f.w[4], f.x[M - 16], a.g2[M - 16]);
    else a.u2[M - 18] = mfma32(f.w[5], f.x[M - 18], a.u2[M - 18]);
}
template <int S, int M> DI void slotA(S1Ctx& c, AccA& a, const FragA& f0, FragA& f1, const AddrA& ad) {
    if constexpr (M < 22) {
        mmA<M>(a, f0);
        if constexpr (S + 1 < NSTEP) rdA<S + 1, M>(f1, c.lds, ad);
        if constexpr (M % 3 == 1) s1_op<true, 2 + S, M / 3>(c);
        FENCE();
        slotA<S, M + 1>(c, a, f0, f1, ad);
    }
}
template <int S, int M> DI void slotB(S1Ctx& c, AccB& a, const FragB& f0, FragB& f1, const AddrB& ad) {
    if constexpr (M < 20) {
        mmB<M>(a, f0);
        if constexpr (S + 1 < NSTEP) rdB<S + 1, M>(f1, c.lds, ad);
        if constexpr (M % 3 == 1) s1_op<false, 2 + S, M / 3>(c);
        FENCE();
        slotB<S, M + 1>(c, a, f0, f1, ad);
    }
}
template <int S> DI void stepA(S1Ctx& c, AccA& a, FragA& f0, FragA& f1, const AddrA& ad) {
    if constexpr (S < NSTEP) { s1_top<true, S>(c); slotA<S, 0>(c, a, f0, f1, ad); stepA<S + 1>(c, a, f1, f0, ad); }
}
template <int S> DI void stepB(S1Ctx& c, AccB& a, FragB& f0, FragB& f1, const AddrB& ad) {
    if constexpr (S < NSTEP) { s1_top<false, S>(c); slotB<S, 0>(c, a, f0, f1, ad); stepB<S + 1>(c, a, f1, f0, ad); }
}
template <int S, int I> DI void rdA_all(FragA& f, LDSP char* lds, const AddrA& a) { if constexpr (I < 15) { rdA<S, I>(f, lds, a); rdA_all<S, I + 1>(f, lds, a); } }
template <int S, int I> DI void rdB_all(FragB& f, LDSP char* lds, const AddrB& a) { if constexpr (I < 16) { rdB<S, I>(f, lds, a); rdB_all<S, I + 1>(f, lds, a); } }

template <bool X> DI void s1_prologue(S1Ctx& c) {
    STAMP(c, 24);
    s1_ops_all<X, 0, 0>(c);
    STAMP(c, 25);
    barrier();
    s1_ops_all<X, 1, 0>(c);
    STAMP(c, 26);
}
DI unsigned opaque(unsigned v) { asm volatile("" : "+v"(v)); return v; }
DI float row_sum16(float v) {
    v += __builtin_bit_cast(float, __builtin_amdgcn_update_dpp(0, __builtin_bit_cast(int, v), 0x128, 0xf, 0xf, false));
    v += __builtin_bit_cast(float, __builtin_amdgcn_update_dpp(0, __builtin_bit_cast(int, v), 0x124, 0xf, 0xf, false));
    v += __builtin_bit_cast(float, __builtin_amdgcn_update_dpp(0, __builtin_bit_cast(int, v), 0x122, 0xf, 0xf, false));
    v += __builtin_bit_cast(float, __builtin_amdgcn_update_dpp(0, __builtin_bit_cast(int, v), 0x121, 0xf, 0xf, false));
    return v;
}
DI float xsum16(float m) { u32x2 r = __builtin_amdgcn_permlane16_swap(__float_as_uint(m), __float_as_uint(m), false, false); return __uint_as_float(r[0]) + __uint_as_float(r[1]); }
DI float xsum32(float m) { u32x2 r = __builtin_amdgcn_permlane32_swap(__float_as_uint(m), __float_as_uint(m), false, false); return __uint_as_float(r[0]) + __uint_as_float(r[1]); }

template <bool X, bool SC1> DI void stage1(const Params& P, LDSP char* lds, int p, int cg) {
    const int tid = threadIdx.x, l = tid & 63;
    const int w = __builtin_amdgcn_readfirstlane(tid >> 6);
    const int r = w >> 2, j = w & 3;
    const int a = l & 15, q = l >> 4;
    S1Ctx cx; cx.lds = lds;
    STAMP(cx, 0);
    cx.xsrd = make_srd(P.x, SEQ * DM * 2);
    {
        const int row = l >> 3, cp = l & 7;
        const int f = ((row >> 1) & 1) | ((j & 1) << 1);
        const unsigned colb = (unsigned)((((cp >> 1) ^ f) << 5) + ((cp & 1) << 4));
        const unsigned vo3072 = row * 3072 + colb, vo3584 = row * 3584 + colb;
        const i32x4 sq = make_srd(P.wqkv, DM * 3 * DM * 2), sg = make_srd(P.wg, DM * FF * 2), su = make_srd(P.wu, DM * FF * 2);
        if constexpr (X) {
            cx.srd[0] = sq; cx.wvo[0] = vo3072; cx.wso[0] = 8 * j * 3072 + r * 1024 + cg * 128; cx.wstr[0] = 32 * 3072; cx.wdst[0] = r * 4096 + j * 1024;
            cx.srd[1] = r ? sg : sq; cx.wvo[1] = r ? vo3584 : vo3072;
            cx.wso[1] = r ? 8 * j * 3584 + 2560 + cg * 128 : 8 * j * 3072 + 2048 + cg * 128;
            cx.wstr[1] = r ? 32 * 3584 : 32 * 3072; cx.wdst[1] = (2 + r) * 4096 + j * 1024;
            const int f2 = ((row >> 1) & 1) | (w & 2);
            cx.srd[2] = su; cx.wvo[2] = (row & 3) * 3584 + ((((cp >> 1) ^ f2) << 5) + ((cp & 1) << 4));
            cx.wso[2] = 4 * w * 3584 + 2560 + cg * 128; cx.wstr[2] = 32 * 3584; cx.wdst[2] = 16384 + w * 512;
        } else {
            const int fullb = cg * 320 + ((cg & 1) ? 64 : 0), halfb = cg * 320 + ((cg & 1) ? 0 : 256);
            cx.srd[0] = sg; cx.wvo[0] = vo3584; cx.wso[0] = 8 * j * 3584 + fullb + r * 128; cx.wstr[0] = 32 * 3584; cx.wdst[0] = r * 4096 + j * 1024;
            cx.srd[1] = su; cx.wvo[1] = vo3584; cx.wso[1] = cx.wso[0]; cx.wstr[1] = 32 * 3584; cx.wdst[1] = (2 + r) * 4096 + j * 1024;
            const int hr = l >> 2, hc = l & 3;
            cx.srd[2] = r ? su : sg;
            cx.wvo[2] = (hr & 7) * 3584 + ((((hc >> 1) ^ (w & 1)) << 5) + ((hc & 1) << 4));
            cx.wso[2] = 8 * j * 3584 + halfb; cx.wstr[2] = 32 * 3584; cx.wdst[2] = 16384 + w * 512;
        }
        const int tok = 128 * p + 32 * (w >> 1) + 2 * row + (w & 1);
        cx.xvo = tok * 1024 + ((cp ^ row) << 4);
        cx.xdst = LDS_X + w * 2048;
        const int ah = 2 * w + (l >> 5), jd = l & 31;
        int ht = 128 * p - 16 + (ah < 8 ? 2 * ah : 2 * (ah - 8) + 1);
        ht = ht < 0 ? 0 : ht;
        cx.hvo = ht * 1024 + (((jd >> 2) ^ (ah & 7)) << 4) + (jd & 3) * 4;
        cx.hdst = LDS_X + 16384 + w * 256;
    }
    const int gtok = 128 * p + 8 * (2 * cg + (X ? 0 : 1)) + w;
    u32x4 gx, gw;
    asm volatile("global_load_dwordx4 %0, %2, off\n\tglobal_load_dwordx4 %1, %3, off"
                 : "=&v"(gx), "=&v"(gw) : "v"(P.x + gtok * 1024 + l * 16), "v"(P.gw + l * 16) : "memory");
    s1_prologue<X>(cx);
    asm volatile("s_waitcnt vmcnt(%2)" : "+v"(gx), "+v"(gw) : "n"(SchedOf<X>::v.n[0] + SchedOf<X>::v.n[1]) : "memory");
    float gl = 0.f;
    for (int i = 0; i < 4; ++i) {
        const unsigned xa_ = gx[i], wa_ = gw[i];
        gl = __builtin_amdgcn_fdot2_f32_bf16(__builtin_bit_cast(bf16x2, xa_), __builtin_bit_cast(bf16x2, wa_), gl, false);
    }
    const float gate = sigmoidf_(xsum32(xsum16(row_sum16(gl))));

    STAMP(cx, 27);
    const int qp = a >> 2, pp = a & 3;
    const int fsw = ((qp >> 1) & 1) | ((q & 1) << 1);
    const unsigned xa0 = LDS_X + (64 * r + a) * 128 + ((q ^ (a & 7)) << 4);
    char* act = P.ws + WS_ACT;
    const int rb = 2 * p + r;
    wait_vm<in_flight_ok(X, 0, 2)>();
    barrier();
    STAMP(cx, 1);
    if constexpr (X) {
        AddrA ad;
        ad.w[0] = (8 * q + qp) * 128 + ((j ^ fsw) << 5) + pp * 8; ad.w[1] = opaque(ad.w[0] + 2 * WSLAB);
        ad.x[0] = xa0; ad.x[1] = xa0 ^ 64u;
        const int hrow = r == 0 ? 128 + a : 40 + a + (a >= 8 ? 8 : 0);
        ad.h[0] = LDS_X + hrow * 128 + ((q ^ (a & 7)) << 4); ad.h[1] = ad.h[0] ^ 64u;
        const float NINF = -__builtin_inff();
        f32x4 bc, bp, bh0, bh1;
        const bool first = (p == 0) && (r == 0);
        for (int i = 0; i < 4; ++i) {
            const int b = 4 * q + i;
            bc[i] = (a - b >= 0 && a - b <= 7) ? 0.f : NINF;
            bp[i] = (b >= a + 9) ? 0.f : NINF;
            bh0[i] = (!first && b >= a + 1 && b <= 7) ? 0.f : NINF;
            bh1[i] = first ? NINF : bp[i];
        }
        asm volatile("" : "+v"(bc), "+v"(bp), "+v"(bh0), "+v"(bh1));
        AccA acc;
        for (int i = 0; i < 4; ++i) { acc.q[i] = 0.f; acc.g[i] = 0.f; acc.u[i] = 0.f; }
        for (int i = 0; i < 5; ++i) { acc.k[i] = 0.f; acc.v[i] = 0.f; }
        FragA f0, f1;
        rdA_all<0, 0>(f0, lds, ad);
        stepA<0>(cx, acc, f0, f1, ad);
        STAMP(cx, 18);
        FENCE();
        {
            const int T = 32 + 80 + 4 * cg + j;
            u32x2 h[4];
            for (int i = 0; i < 4; ++i) h[i] = pk4(glu4(acc.g[i], acc.u[i]));
            for (int tg = 0; tg < 2; ++tg)
                st16<SC1>(act, (unsigned)(((rb * NKT + T) * 2 + tg) * 1024 + l * 16), frag32(h[2 * tg], h[2 * tg + 1]));
        }
        const float c2 = 0.25f * 1.4426950408889634f;
        u32x2 qb[4], kb[5], vb[5], ob[4];
        for (int i = 0; i < 4; ++i) qb[i] = pk4(acc.q[i]);
        for (int i = 0; i < 5; ++i) kb[i] = pk4(acc.k[i]);
        FENCE();
        f32x4 sp[4], sc[4];
        for (int R = 0; R < 4; ++R) {
            sp[R] = mfma16(kb[R >= 2 ? R - 2 : 4], qb[R], R == 0 ? bh0 : R == 1 ? bh1 : bp);
            sc[R] = mfma16(kb[R], qb[R], bc);
        }
        FENCE();
        for (int i = 0; i < 5; ++i) vb[i] = pk4(acc.v[i]);
        FENCE();
        float m[4];
        for (int R = 0; R < 4; ++R) m[R] = max8(sp[R], sc[R]);
        colmax4(m[0], m[1], m[2], m[3]);
        for (int R = 0; R < 4; ++R) m[R] *= -c2;
        FENCE();
        const u32x4 onesu = {0x3f803f80u, 0x3f803f80u, 0x3f803f80u, 0x3f803f80u};
        const bf16x8 ones = __builtin_bit_cast(bf16x8, onesu);
        const f32x4 z4 = {0.f, 0.f, 0.f, 0.f};
        f32x4 o[4], sm[4];
        for (int R = 0; R < 4; ++R) {
            for (int i = 0; i < 4; ++i) {
                sp[R][i] = __builtin_amdgcn_exp2f(__builtin_fmaf(sp[R][i], c2, m[R]));
                sc[R][i] = __builtin_amdgcn_exp2f(__builtin_fmaf(sc[R][i], c2, m[R]));
            }
            const bf16x8 pf = cat8(pk4(sp[R]), pk4(sc[R]));
            o[R] = mfma32(cat8(vb[R >= 2 ? R - 2 : 4], vb[R]), pf, z4);
            sm[R] = mfma32(ones, pf, z4);
            FENCE();
        }
        for (int R = 0; R < 4; ++R) ob[R] = pk4(o[R] * __builtin_amdgcn_rcpf(sm[R][0]));
        const int T = 4 * cg + j;
        for (int tg = 0; tg < 2; ++tg)
            st16<SC1>(act, (unsigned)(((rb * NKT + T) * 2 + tg) * 1024 + l * 16), frag32(ob[2 * tg], ob[2 * tg + 1]));
    } else {
        AddrB ad;
        ad.w[0][0] = (j >> 1) * 4096 + (8 * q + qp) * 128 + (((2 * (j & 1)) ^ fsw) << 5) + pp * 8;
        ad.w[0][1] = ad.w[0][0] ^ 32u;
        ad.wh[0] = 16384 + (8 * q + qp) * 64 + (((j >> 1) ^ (q & 1)) << 5) + pp * 8;
        ad.w[1][0] = opaque(ad.w[0][0] + 2 * WSLAB); ad.w[1][1] = opaque(ad.w[0][1] + 2 * WSLAB); ad.wh[1] = opaque(ad.wh[0] + 2 * WSLAB);
        const unsigned rot = 4096u * (j & 1);
        ad.x[0] = xa0 + rot; ad.x[1] = (xa0 ^ 64u) + rot; ad.x[2] = xa0 - rot; ad.x[3] = (xa0 ^ 64u) - rot;
        AccB acc;
        for (int i = 0; i < 4; ++i) { acc.g0[i] = 0.f; acc.u0[i] = 0.f; acc.g1[i] = 0.f; acc.u1[i] = 0.f; }
        for (int i = 0; i < 2; ++i) { acc.g2[i] = 0.f; acc.u2[i] = 0.f; }
        FragB f0, f1;
        rdB_all<0, 0>(f0, lds, ad);
        stepB<0>(cx, acc, f0, f1, ad);
        STAMP(cx, 18);
        FENCE();
        const int fbase = 32 + 10 * cg;
        const int T0 = fbase + ((cg & 1) ? 2 : 0) + 2 * j, T2 = fbase + ((cg & 1) ? 0 : 8) + (j >> 1);
        u32x2 h0[4], h1[4], h2[2];
        for (int i = 0; i < 4; ++i) { h0[i] = pk4(glu4(acc.g0[i], acc.u0[i])); h1[i] = pk4(glu4(acc.g1[i], acc.u1[i])); }
        for (int i = 0; i < 2; ++i) h2[i] = pk4(glu4(acc.g2[i], acc.u2[i]));
        for (int i = 0; i < 2; ++i) {
            const int tg = i ^ (j & 1);
            st16<SC1>(act, (unsigned)(((rb * NKT + T0) * 2 + tg) * 1024 + l * 16), frag32(h0[2 * i], h0[2 * i + 1]));
            st16<SC1>(act, (unsigned)(((rb * NKT + T0 + 1) * 2 + tg) * 1024 + l * 16), frag32(h1[2 * i], h1[2 * i + 1]));
        }
        st16<SC1>(act, (unsigned)(((rb * NKT + T2) * 2 + (j & 1)) * 1024 + l * 16), frag32(h2[0], h2[1]));
    }
    if (l == 0) ((float*)(P.ws + WS_G))[gtok] = gate;
}

__global__ __launch_bounds__(512, 2) void k_s1(Params P) {
    extern __shared__ __attribute__((aligned(16))) char smem[];
    const int b = blockIdx.x, xl = b & 7, idx = b >> 3;
    const int p = 8 * (xl & 1) + (idx & 7), gi = idx >> 3, cg = 2 * (xl >> 1) + (gi & 1);
    if (gi < 2) stage1<true, false>(P, (LDSP char*)smem, p, cg);
    else stage1<false, false>(P, (LDSP char*)smem, p, cg);
}

constexpr int imin(int a, int b) { return a < b ? a : b; }
constexpr int S2_CH = 4, S2_NCH = 9, S2_RING = 5, S2_CHB = 4 * S2_CH * 2048;
DI constexpr int s2_tile(int kq, int i) { return i < 8 ? 8 * kq + i : 32 + 28 * kq + (i - 8); }
DI f32x16 mfma3232(bf16x8 a, bf16x8 b, f32x16 c) { return __builtin_amdgcn_mfma_f32_32x32x16_bf16(a, b, c, 0, 0, 0); }

struct S2Ctx {
    LDSP char* lds;
    i32x4 wosrd, wdsrd, asrd;
    unsigned wvo, avo;
    int nh, kq, w;
};
template <int I0> DI void s2_issue_w(const S2Ctx& c) {
#pragma unroll
    for (int i = 0; i < 18; ++i) {
        const int li = I0 + i;
        const unsigned dst = (unsigned)((c.kq * 18 + i) * 2048 + c.nh * 1024);
        if (li < 8) dma16b(c.wosrd, c.wvo, (unsigned)((8 * c.kq + li) * 16 * 1024), dst);
        else dma16b(c.wdsrd, c.wvo, (unsigned)((28 * c.kq + li - 8) * 16 * 1024), dst);
    }
}
template <int CH> DI void s2_issue_a(const S2Ctx& c) {
    if constexpr (CH < S2_NCH) {
#pragma unroll
        for (int i = 0; i < S2_CH; ++i) {
            const int li = CH * S2_CH + i;
            const int T = li < 8 ? 8 * c.kq + li : 32 + 28 * c.kq + (li - 8);
            dma16b(c.asrd, c.avo, (unsigned)(T * 2048), (unsigned)((CH % S2_RING) * S2_CHB + (c.kq * S2_CH + i) * 2048 + c.nh * 1024));
        }
    }
}
constexpr int s2_wait(int ch) { const int last = imin(ch + S2_RING - 2, S2_NCH - 1); return S2_CH * (last - ch); }
template <int CH> DI void s2_chunk(const S2Ctx& c, const bf16x8 (&wf)[36], f32x16 (&accA)[2], f32x16 (&accB)[2], unsigned ard) {
    if constexpr (CH < S2_NCH) {
        wait_vm<s2_wait(CH)>();
        wait_lgkm0();
        barrier();
        s2_issue_a<CH + S2_RING - 1>(c);
#pragma unroll
        for (int i = 0; i < S2_CH; ++i) {
            constexpr unsigned base = (unsigned)(CH % S2_RING) * S2_CHB;
            const int li = CH * S2_CH + i;
            const bf16x8 a0 = lds_row(c.lds, ard + base + i * 2048), a1 = lds_row(c.lds, ard + base + i * 2048 + 1024);
            if (li < 8) { accA[0] = mfma3232(wf[li], a0, accA[0]); accA[1] = mfma3232(wf[li], a1, accA[1]); }
            else { accB[0] = mfma3232(wf[li], a0, accB[0]); accB[1] = mfma3232(wf[li], a1, accB[1]); }
        }
        s2_chunk<CH + 1>(c, wf, accA, accB, ard);
    }
}

DI void stage2(const Params& P, LDSP char* lds, int rb, int nb) {
    const int tid = threadIdx.x, l = tid & 63;
    const int w = __builtin_amdgcn_readfirstlane(tid >> 6);
    S2Ctx c; c.lds = lds; c.w = w; c.nh = w & 1; c.kq = w >> 1;
    c.wosrd = make_srd(P.wo, DM * DM * 2);
    c.wdsrd = make_srd(P.wd, FF * DM * 2);
    c.asrd = make_srd(P.ws + WS_ACT + (size_t)rb * NKT * 2048, NKT * 2048);
    {
        const int row = l >> 3, cp = l & 7, hf = (cp >> 2) ^ ((row >> 1) & 1);
        c.wvo = (unsigned)((8 * c.nh + row) * 1024 + nb * 128 + hf * 64 + (cp & 3) * 16);
        c.avo = (unsigned)(c.nh * 1024 + l * 16);
    }
    const int h = l >> 5, blk = (l >> 4) & 1, qp = (l & 15) >> 2, pp = l & 3;
    const int wrow = 8 * h + qp;
    const unsigned wrd = (unsigned)(c.kq * 18 * 2048 + wrow * 128 + ((c.nh ^ ((wrow >> 1) & 1)) << 6) + blk * 32 + pp * 8);
    bf16x8 wf[36];

    s2_issue_w<0>(c);
    wait_vm<0>();
    barrier();
#pragma unroll
    for (int i = 0; i < 18; ++i) wf[i] = lds_tr(lds, wrd + i * 2048, 512);
    wait_lgkm0();
    barrier();
    s2_issue_w<18>(c);
    wait_vm<0>();
    barrier();
#pragma unroll
    for (int i = 0; i < 18; ++i) wf[18 + i] = lds_tr(lds, wrd + i * 2048, 512);
    wait_lgkm0();
    barrier();

    s2_issue_a<0>(c); s2_issue_a<1>(c); s2_issue_a<2>(c); s2_issue_a<3>(c);
    const int pos = l & 31;
    const int tokp = pos < 16 ? 2 * pos : 2 * (pos - 16) + 1;
    f32x16 accA[2], accB[2];
    for (int i = 0; i < 16; ++i) { accA[0][i] = 0.f; accA[1][i] = 0.f; accB[0][i] = 0.f; accB[1][i] = 0.f; }
    s2_chunk<0>(c, wf, accA, accB, (unsigned)(c.kq * S2_CH * 2048 + l * 16));

    const float* gbuf = (const float*)(P.ws + WS_G) + 64 * rb + tokp;
    const float g0 = gbuf[0], g1 = gbuf[32];
    wait_lgkm0();
    barrier();
#pragma unroll
    for (int tg = 0; tg < 2; ++tg) {
        const float g = tg ? g1 : g0, gm = 1.f - g;
        const int row = tg * 32 + pos;
#pragma unroll
        for (int gi = 0; gi < 4; ++gi) {
            f32x4 v;
            for (int j = 0; j < 4; ++j) v[j] = g * accA[tg][4 * gi + j] + gm * accB[tg][4 * gi + j];
            const int ch = c.nh * 8 + 2 * gi + h;
            *(LDSP f32x4*)(lds + c.kq * 16384 + row * 256 + ((ch ^ (row & 7)) << 4)) = v;
        }
    }
    wait_lgkm0();
    barrier();
    {
        const int row = tid >> 3, c8 = tid & 7;
        f32x4 s0 = {0.f, 0.f, 0.f, 0.f}, s1 = s0;
#pragma unroll
        for (int k = 0; k < 4; ++k) {
            s0 += *(LDSP f32x4*)(lds + k * 16384 + row * 256 + (((2 * c8) ^ (row & 7)) << 4));
            s1 += *(LDSP f32x4*)(lds + k * 16384 + row * 256 + (((2 * c8 + 1) ^ (row & 7)) << 4));
        }
        const int ps = row & 31;
        const int tok = 64 * rb + (row & 32) + (ps < 16 ? 2 * ps : 2 * (ps - 16) + 1);
        const u32x2 a = pk4(s0), b = pk4(s1);
        const u32x4 o = {a[0], a[1], b[0], b[1]};
        *(u32x4*)(P.out + (size_t)tok * 1024 + nb * 128 + c8 * 16) = o;
    }
}

__global__ __launch_bounds__(512, 2) void k_s2(Params P) {
    extern __shared__ __attribute__((aligned(16))) char smem[];
    const int b = blockIdx.x, xl = b & 7, idx = b >> 3;
    stage2(P, (LDSP char*)smem, 4 * xl + (idx >> 3), idx & 7);
}

}

extern "C" void kernel_launch(void* const* d_in, const int* in_sizes, int n_in, void* d_out, int out_size, void* d_ws, size_t ws_size,
                              hipStream_t stream) {
    Params P;
    P.x = (const char*)d_in[0]; P.wqkv = (const char*)d_in[1]; P.wo = (const char*)d_in[2];
    P.wg = (const char*)d_in[4]; P.wu = (const char*)d_in[5]; P.wd = (const char*)d_in[6]; P.gw = (const char*)d_in[7];
    P.out = (char*)d_out; P.ws = (char*)d_ws;
    (void)hipFuncSetAttribute((const void*)k_s1, hipFuncAttributeMaxDynamicSharedMemorySize, LDS_TOTAL);
    (void)hipFuncSetAttribute((const void*)k_s2, hipFuncAttributeMaxDynamicSharedMemorySize, LDS_TOTAL);
    k_s1<<<256, 512, LDS_TOTAL, stream>>>(P);
    k_s2<<<256, 512, LDS_TOTAL, stream>>>(P);
}
```

```cpp
#include <hip/hip_runtime.h>
#include <hip/hip_bf16.h>

#define LDSP __attribute__((address_space(3)))
#define GLBP __attribute__((address_space(1)))
#define DI __device__ __forceinline__
typedef short s16x4 __attribute__((ext_vector_type(4)));
typedef __bf16 bf16x8 __attribute__((ext_vector_type(8)));
typedef __bf16 bf16x2 __attribute__((ext_vector_type(2)));
typedef float f32x2 __attribute__((ext_vector_type(2)));
typedef float f32x4 __attribute__((ext_vector_type(4)));
typedef float f32x16 __attribute__((ext_vector_type(16)));
typedef unsigned u32x2 __attribute__((ext_vector_type(2)));
typedef unsigned u32x4 __attribute__((ext_vector_type(4)));
typedef int i32x4 __attribute__((ext_vector_type(4)));

namespace {

constexpr int SEQ = 2048, DM = 512, FF = 1792;
constexpr int WSLAB = 32 * 640;
constexpr int XSLAB = 144 * 128;
constexpr int NWS = 5, NXS = 3;
constexpr int LDS_X = NWS * WSLAB;
constexpr int LDS_S1 = LDS_X + NXS * XSLAB;
constexpr int LDS_TOTAL = 163840;
constexpr int CODE_LINES = 220;
constexpr int AUX_SC1 = 16;
constexpr int NKT = 144;
constexpr size_t ACT_BYTES = (size_t)SEQ * NKT * 16 * 2;
constexpr size_t WS_ACT = 0, WS_G = ACT_BYTES, WS_SYNC = WS_G + 8192;
constexpr int SYNC_STRIDE = 512;

DI void dma4g(const void* g, unsigned lds) {
    asm volatile("s_mov_b32 m0, %1\n\ts_nop 0\n\tglobal_load_lds_dword %0, off" ::"v"(g), "s"(lds) : "memory");
}
DI void dma16b(i32x4 srd, unsigned voff, unsigned soff, unsigned lds) {
    asm volatile("s_mov_b32 m0, %3\n\ts_nop 0\n\tbuffer_load_dwordx4 %0, %1, %2 offen lds" ::"v"(voff), "s"(srd), "s"(soff), "s"(lds) : "memory");
}
DI void dma16b_rf(i32x4 srd, unsigned voff, unsigned soff, unsigned lds) {
    asm volatile("s_nop 4\n\ts_mov_b32 m0, %3\n\ts_nop 0\n\tbuffer_load_dwordx4 %0, %1, %2 offen lds"
                 ::"v"(voff), "s"(srd), "s"(__builtin_amdgcn_readfirstlane(soff)), "s"(__builtin_amdgcn_readfirstlane(lds)) : "memory");
}
DI void dma16b_half(i32x4 srd, unsigned voff, unsigned soff, unsigned lds) {
    asm volatile("s_mov_b32 exec_hi, 0\n\ts_mov_b32 m0, %3\n\ts_nop 0\n\tbuffer_load_dwordx4 %0, %1, %2 offen lds\n\ts_mov_b32 exec_hi, -1"
                 ::"v"(voff), "s"(srd), "s"(soff), "s"(lds) : "memory");
}
DI void dma4b(i32x4 srd, unsigned voff, unsigned soff, unsigned lds) {
    asm volatile("s_mov_b32 m0, %3\n\ts_nop 0\n\tbuffer_load_dword %0, %1, %2 offen lds" ::"v"(voff), "s"(srd), "s"(soff), "s"(lds) : "memory");
}
template <int N> DI void wait_vm() { asm volatile("s_waitcnt vmcnt(%0)" ::"n"(N) : "memory"); }
DI void wait_lgkm0() { asm volatile("s_waitcnt lgkmcnt(0)" ::: "memory"); }
DI void barrier() { asm volatile("" ::: "memory"); __builtin_amdgcn_s_barrier(); asm volatile("" ::: "memory"); }
DI i32x4 make_srd(const void* p, unsigned bytes) {
    const unsigned long long a = (unsigned long long)p;
    i32x4 r; r[0] = (int)(unsigned)a; r[1] = (int)((unsigned)(a >> 32) & 0xffffu); r[2] = (int)bytes; r[3] = 0x00020000; return r;
}
DI i32x4 make_srd_s(const void* p, unsigned bytes) {
    i32x4 r = make_srd(p, bytes);
    int a = __builtin_amdgcn_readfirstlane(r[0]), b = __builtin_amdgcn_readfirstlane(r[1]);
    asm volatile("s_nop 4" : "+s"(a), "+s"(b));
    r[0] = a; r[1] = b;
    return r;
}
DI unsigned pk2(float a, float b) { f32x2 v = {a, b}; return __builtin_bit_cast(unsigned, __builtin_convertvector(v, bf16x2)); }
DI u32x2 pk4(f32x4 v) { u32x2 r; r[0] = pk2(v[0], v[1]); r[1] = pk2(v[2], v[3]); return r; }
DI bf16x8 cat8(u32x2 a, u32x2 b) { u32x4 r = {a[0], a[1], b[0], b[1]}; return __builtin_bit_cast(bf16x8, r); }
DI f32x4 mfma32(bf16x8 a, bf16x8 b, f32x4 c) { return __builtin_amdgcn_mfma_f32_16x16x32_bf16(a, b, c, 0, 0, 0); }
DI f32x4 mfma16(u32x2 a, u32x2 b, f32x4 c) {
    return __builtin_amdgcn_mfma_f32_16x16x16bf16_1k(__builtin_bit_cast(s16x4, a), __builtin_bit_cast(s16x4, b), c, 0, 0, 0);
}
DI bf16x8 lds_row(LDSP char* base, unsigned off) { return *(LDSP bf16x8*)(base + off); }
DI bf16x8 lds_tr(LDSP char* base, unsigned off, unsigned step) {
    s16x4 a = __builtin_amdgcn_ds_read_tr16_b64_v4i16((LDSP s16x4*)(base + off));
    s16x4 b = __builtin_amdgcn_ds_read_tr16_b64_v4i16((LDSP s16x4*)(base + off + step));
    return __builtin_bit_cast(bf16x8, __builtin_shufflevector(a, b, 0, 1, 2, 3, 4, 5, 6, 7));
}
DI float max8(f32x4 a, f32x4 b) {
    float m;
    asm volatile("v_max3_f32 %0, %1, %2, %3\n\tv_max3_f32 %0, %0, %4, %5\n\tv_max3_f32 %0, %0, %6, %7\n\tv_max_f32 %0, %0, %8"
                 : "=&v"(m) : "v"(a[0]), "v"(a[1]), "v"(a[2]), "v"(a[3]), "v"(b[0]), "v"(b[1]), "v"(b[2]), "v"(b[3]));
    return m;
}
DI void colmax4(float& m0, float& m1, float& m2, float& m3) {
    asm volatile("s_nop 1\n\t"
                 "v_permlane16_swap_b32 %0, %1\n\tv_permlane16_swap_b32 %2, %3\n\t"
                 "v_max_f32 %0, %0, %1\n\tv_max_f32 %2, %2, %3\n\ts_nop 1\n\t"
                 "v_permlane32_swap_b32 %0, %2\n\t"
                 "v_max_f32 %0, %0, %2\n\t"
                 "v_mov_b32 %2, %0\n\ts_nop 1\n\t"
                 "v_permlane32_swap_b32 %0, %2\n\t"
                 "v_mov_b32 %1, %0\n\tv_mov_b32 %3, %2\n\ts_nop 1\n\t"
                 "v_permlane16_swap_b32 %0, %1\n\tv_permlane16_swap_b32 %2, %3\n\ts_nop 1"
                 : "+v"(m0), "+v"(m1), "+v"(m2), "+v"(m3));
}
DI u32x4 frag32(u32x2 e, u32x2 o) {
    u32x2 a = __builtin_amdgcn_permlane16_swap(e[0], o[0], false, false);
    u32x2 b = __builtin_amdgcn_permlane16_swap(e[1], o[1], false, false);
    u32x4 r = {a[0], b[0], a[1], b[1]};
    return r;
}
DI float sigmoidf_(float g) { return __builtin_amdgcn_rcpf(1.f + __builtin_amdgcn_exp2f(g * -1.4426950408889634f)); }
DI void glu_scale(f32x4& g) { g *= -1.4426950408889634f; }
DI void glu_exp(f32x4& g) { for (int j = 0; j < 4; ++j) g[j] = __builtin_amdgcn_exp2f(g[j]); }
DI void glu_add(f32x4& g) { g += 1.f; }
DI void glu_rcp(f32x4& g) { for (int j = 0; j < 4; ++j) g[j] = __builtin_amdgcn_rcpf(g[j]); }

struct Params {
    const char* x; const char* wqkv; const char* wo; const char* wg; const char* wu; const char* wd; const char* gw;
    char* out; char* ws;
};

#define FENCE() __builtin_amdgcn_sched_barrier(0)

DI void st16(char* base, unsigned off, u32x4 v) {
    auto r = __builtin_amdgcn_make_buffer_rsrc((void*)base, 0, 0x7fffffff, 0x00020000);
    __builtin_amdgcn_raw_buffer_store_b128(v, r, off, 0, AUX_SC1);
}

constexpr int NSTEP = 16;
constexpr int NPHASE = 2 + NSTEP, MAXOPS = 9;
struct Sched { int n[NPHASE]; int op[NPHASE][MAXOPS]; };
constexpr int OP_TOUCH_CODE = 192, OP_TOUCH_W2 = 196, OP_TOUCH_LAST = 200;
constexpr int opw(int slab, int part) { return 4 * slab + part; }
constexpr int opx(int slab, int part) { return 64 + 4 * slab + part; }
constexpr Sched make_sched(bool X) {
    Sched s{};
    auto addw = [&](int ph, int slab) { for (int p = 0; p < 3; ++p) s.op[ph][s.n[ph]++] = opw(slab, p); };
    auto addx = [&](int ph, int slab, int p0, int p1) { for (int p = p0; p <= p1; ++p) if (p < 2 || X) s.op[ph][s.n[ph]++] = opx(slab, p); };
    addx(0, 0, 0, 2); addw(0, 0);
    addw(1, 1); addx(1, 1, 0, 2);
    addw(2, 2); addw(2, 3);
    addw(3, 4); addx(3, 2, 0, 2);
    addw(4, 5); addw(4, 6);
    addw(5, 7); addx(5, 3, 0, 2);
    addw(6, 8); addw(6, 9);
    addw(7, 10); addx(7, 4, 0, 2);
    addw(8, 11); addx(8, 5, 0, 2);
    for (int T = 7; T <= 10; ++T) {
        addw(2 + T, T + 5);
        if (T & 1) addx(2 + T, (T - 1) / 2 + 3, 0, 0); else addx(2 + T, T / 2 + 2, 1, 2);
    }
    s.op[2 + 0][s.n[2 + 0]++] = OP_TOUCH_CODE;
    s.op[2 + 6][s.n[2 + 6]++] = OP_TOUCH_W2;
    s.op[2 + 12][s.n[2 + 12]++] = OP_TOUCH_LAST;
    const int r0[6] = {0, 4, 6, 10, 12, 16};
    for (int T = 11; T <= 15; ++T) for (int i = r0[T - 11]; i < r0[T - 10]; ++i) s.op[2 + T][s.n[2 + T]++] = 128 + 4 * i;
    return s;
}
template <bool X> struct SchedOf { static constexpr Sched v = make_sched(X); };
constexpr int in_flight_ok(bool X, int s, int ph_end) {
    const Sched sc = make_sched(X);
    int n = 0, need = 0;
    for (int ph = 0; ph < ph_end; ++ph)
        for (int i = 0; i < sc.n[ph]; ++i) {
            const int o = sc.op[ph][i], slab = (o & 63) >> 2;
            ++n;
            if (s < NSTEP ? (o < 64 ? slab == s : o < 128 ? slab == (s >> 1) : false) : (o >= 128 && ((o - 128) >> 2) < 10)) need = n;
        }
    return n - need;
}

struct S1Ctx {
    LDSP char* lds;
    i32x4 xsrd, srd[3];
    unsigned wvo[3], wso[3], wstr[3], wdst[3];
    unsigned xvo, hvo, xdst, hdst;
    const char* code; unsigned codevo;
    const char *touch, *touch2;
    unsigned rot;
    i32x4 wosrd, wdsrd;
    unsigned w2vo, w2dst;
    int nb;
    unsigned kq, mh, wl;
};
DI unsigned s2_T(const S1Ctx& c, int i) {
    return i < 20 ? 32u + 20u * c.kq + i : i < 28 ? 32u + 80u + 8u * c.kq + (i - 20) : 8u * c.kq + (i - 28);
}
DI void issue_w2(const S1Ctx& c, int i, unsigned dst) {
    if (i < 28) dma16b(c.wdsrd, c.w2vo, (s2_T(c, i) - 32u) * 16384u, dst);
    else dma16b(c.wosrd, c.w2vo, s2_T(c, i) * 16384u, dst);
}
constexpr unsigned r_region(int rho) {
    return rho == 0 ? LDS_X + 2 * XSLAB : rho == 1 ? WSLAB : rho == 2 ? 2 * WSLAB : rho == 3 ? LDS_X : rho == 4 ? 3 * WSLAB : rho == 5 ? 4 * WSLAB : rho == 6 ? 0 : LDS_X + XSLAB;
}
constexpr unsigned r_cell(int i) { return r_region(i >> 1) + (i & 1) * 8192; }
constexpr int NR = 16, NR1 = 10, NL = 20, NL1 = 14;
constexpr int ADEPTH = 6;
struct S2Regs { bf16x8 wr[NR][2]; bf16x8 a[ADEPTH]; f32x16 accA[2], accB[2]; };
DI unsigned l_cell(int i, unsigned kq) {
    const int n = i - NR;
    if (n < NR1) return r_cell(n) + kq * 2048u;
    if (n == NR1) return kq * WSLAB + 16384u;
    if (n == NR1 + 1) return kq * WSLAB + 18432u;
    if (n == NR1 + 2) return kq < 2 ? 4 * WSLAB + 16384u + kq * 2048u : LDS_X + 16384u + (kq - 2) * XSLAB;
    if (n == NR1 + 3) return LDS_X + 2 * XSLAB + 16384u + kq * 2048u;
    return r_cell(n - 4) + kq * 2048u;
}
template <int N> DI void issue_w(const S1Ctx& c, int slab) {
    const unsigned so = c.wso[N] + ((slab + 2 * c.rot) & 15u) * c.wstr[N], dst = (unsigned)(slab % NWS) * WSLAB + c.wdst[N];
    if constexpr (N == 2) dma16b_half(c.srd[N], c.wvo[N], so, dst);
    else dma16b(c.srd[N], c.wvo[N], so, dst);
}
template <int PART> DI void issue_x(const S1Ctx& c, int xs) {
    const unsigned slot = (unsigned)(xs % NXS) * XSLAB;
    const unsigned ko = ((xs + c.rot) & 7u) * 128u;
    if constexpr (PART == 2) dma4b(c.xsrd, c.hvo, ko, slot + c.hdst);
    else dma16b(c.xsrd, c.xvo, ko + PART * 16384u, slot + c.xdst + PART * 1024u);
}
template <bool X, int PH, int J> DI void s1_op(const S1Ctx& c) {
    if constexpr (J < SchedOf<X>::v.n[PH]) {
        constexpr int o = SchedOf<X>::v.op[PH][J], slab = (o & 63) >> 2, part = o & 3;
        if constexpr (o < 64) issue_w<part>(c, slab);
        else if constexpr (o < 128) issue_x<part>(c, slab);
        else if constexpr (o == OP_TOUCH_LAST) dma4g(c.touch2, (unsigned)LDS_S1 + 512u);
        else if constexpr (o == OP_TOUCH_W2) dma4g(c.touch, (unsigned)LDS_S1 + 256u);
        else if constexpr (o == OP_TOUCH_CODE) dma4b(make_srd(c.code, CODE_LINES * 128), c.codevo, 0u, (unsigned)LDS_S1);
        else issue_w2(c, (o - 128) >> 2, r_cell((o - 128) >> 2) + c.w2dst);
    }
}
template <bool X, int PH, int J> DI void s1_ops_all(const S1Ctx& c) {
    if constexpr (J < SchedOf<X>::v.n[PH]) { s1_op<X, PH, J>(c); s1_ops_all<X, PH, J + 1>(c); }
}
template <bool X, int S> DI void s1_top(S1Ctx& c) {
    wait_vm<in_flight_ok(X, S + 1, 2 + S)>();
    wait_lgkm0();
    barrier();
    FENCE();
}

struct FragA { bf16x8 x[5], w[5]; };
struct FragB { bf16x8 x[4], w[6]; };
struct AddrA { unsigned w[2], x[2], h[2]; };
struct AddrB { unsigned w[2][2], wh[2], x[4]; };
struct AccA { f32x4 q[4], k[5], v[5], g[4], u[4]; };
struct AccB { f32x4 g0[4], u0[4], g1[4], u1[4], g2[2], u2[2]; };

DI s16x4 lds_tr1(LDSP char* base, unsigned off) { return __builtin_amdgcn_ds_read_tr16_b64_v4i16((LDSP s16x4*)(base + off)); }
DI void set_half(bf16x8& d, s16x4 v, int hi) {
    u32x4 t = __builtin_bit_cast(u32x4, d); const u32x2 s = __builtin_bit_cast(u32x2, v);
    t[2 * hi] = s[0]; t[2 * hi + 1] = s[1]; d = __builtin_bit_cast(bf16x8, t);
}
template <int S, int I> DI void rdA(FragA& f, LDSP char* lds, const AddrA& a) {
    constexpr int hi = (S % NWS) >= 2 ? 1 : 0;
    constexpr unsigned ws = (unsigned)(S % NWS - 2 * hi) * WSLAB, xs = (unsigned)((S >> 1) % NXS) * XSLAB;
    if constexpr (I < 4) f.x[I] = lds_row(lds, a.x[S & 1] + xs + I * 2048);
    else if constexpr (I == 4) f.x[4] = lds_row(lds, a.h[S & 1] + xs);
    else if constexpr (I < 15) { constexpr int n = (I - 5) >> 1, t = (I - 5) & 1; set_half(f.w[n], lds_tr1(lds, a.w[hi] + ws + n * 4096 + t * 512), t); }
}
template <int S, int I> DI void rdB(FragB& f, LDSP char* lds, const AddrB& a) {
    constexpr int hi = (S % NWS) >= 2 ? 1 : 0;
    constexpr unsigned ws = (unsigned)(S % NWS - 2 * hi) * WSLAB, xs = (unsigned)((S >> 1) % NXS) * XSLAB;
    if constexpr (I < 4) f.x[I] = lds_row(lds, a.x[2 * (I >> 1) + (S & 1)] + xs + I * 2048);
    else if constexpr (I < 12) { constexpr int n = (I - 4) >> 1, t = (I - 4) & 1; set_half(f.w[n], lds_tr1(lds, a.w[hi][n >> 1] + ws + (n & 1) * 8192 + t * 512), t); }
    else if constexpr (I < 16) { constexpr int n = (I - 12) >> 1, t = (I - 12) & 1; set_half(f.w[4 + n], lds_tr1(lds, a.wh[hi] + ws + n * 2048 + t * 256), t); }
}
template <int M> DI void mmA(AccA& a, const FragA& f) {
    if constexpr (M < 5) a.k[M] = mfma32(f.w[1], f.x[M], a.k[M]);
    else if constexpr (M < 10) a.v[M - 5] = mfma32(f.x[M - 5], f.w[2], a.v[M - 5]);
    else if constexpr (M < 14) a.q[M - 10] = mfma32(f.w[0], f.x[M - 10], a.q[M - 10]);
    else if constexpr (M < 18) a.g[M - 14] = mfma32(f.w[3], f.x[M - 14], a.g[M - 14]);
    else a.u[M - 18] = mfma32(f.w[4], f.x[M - 18], a.u[M - 18]);
}
template <int M> DI void mmB(AccB& a, const FragB& f) {
    if constexpr (M < 4) a.g0[M] = mfma32(f.w[0], f.x[M], a.g0[M]);
    else if constexpr (M < 8) a.u0[M - 4] = mfma32(f.w[1], f.x[M - 4], a.u0[M - 4]);
    else if constexpr (M < 12) a.g1[M - 8] = mfma32(f.w[2], f.x[M - 8], a.g1[M - 8]);
    else if constexpr (M < 16) a.u1[M - 12] = mfma32(f.w[3], f.x[M - 12], a.u1[M - 12]);
    else if constexpr (M < 18) a.g2[M - 16] = mfma32(f.w[4], f.x[M - 16], a.g2[M - 16]);
    else a.u2[M - 18] = mfma32(f.w[5], f.x[M - 18], a.u2[M - 18]);
}
template <int N> DI void rd_r1(const S1Ctx& c, S2Regs& g) {
    if constexpr (N < 4 * NR1) {
        constexpr int i = N >> 2, nt = (N >> 1) & 1, t = N & 1;
        set_half(g.wr[i][nt], lds_tr1(c.lds, (nt ? c.wl ^ 64u : c.wl) + c.kq * 2048u + r_cell(i) + t * 512), t);
    }
}
template <int S, int M> DI void slotA(S1Ctx& c, S2Regs& g, AccA& a, const FragA& f0, FragA& f1, const AddrA& ad) {
    if constexpr (M < 22) {
        mmA<M>(a, f0);
        if constexpr (S + 1 < NSTEP) rdA<S + 1, M>(f1, c.lds, ad);
        else { rd_r1<2 * M>(c, g); rd_r1<2 * M + 1>(c, g); }
        if constexpr (M % 3 == 1) s1_op<true, 2 + S, M / 3>(c);
        FENCE();
        slotA<S, M + 1>(c, g, a, f0, f1, ad);
    }
}
template <int S, int M> DI void slotB(S1Ctx& c, S2Regs& g, AccB& a, const FragB& f0, FragB& f1, const AddrB& ad) {
    if constexpr (M < 20) {
        mmB<M>(a, f0);
        if constexpr (S + 1 < NSTEP) rdB<S + 1, M>(f1, c.lds, ad);
        else { rd_r1<2 * M>(c, g); rd_r1<2 * M + 1>(c, g); }
        if constexpr (M % 3 == 1) s1_op<false, 2 + S, M / 3>(c);
        FENCE();
        slotB<S, M + 1>(c, g, a, f0, f1, ad);
    }
}
template <int S> DI void stepA(S1Ctx& c, S2Regs& g, AccA& a, FragA& f0, FragA& f1, const AddrA& ad) {
    if constexpr (S < NSTEP) { s1_top<true, S>(c); slotA<S, 0>(c, g, a, f0, f1, ad); stepA<S + 1>(c, g, a, f1, f0, ad); }
}
template <int S> DI void stepB(S1Ctx& c, S2Regs& g, AccB& a, FragB& f0, FragB& f1, const AddrB& ad) {
    if constexpr (S < NSTEP) { s1_top<false, S>(c); slotB<S, 0>(c, g, a, f0, f1, ad); stepB<S + 1>(c, g, a, f1, f0, ad); }
}
template <int N> DI void issue_l(const S1Ctx& c) { FENCE(); issue_w2(c, NR + N, l_cell(NR + N, c.kq) + c.mh * 1024u); FENCE(); }
template <int S, int I> DI void rdA_all(FragA& f, LDSP char* lds, const AddrA& a) { if constexpr (I < 15) { rdA<S, I>(f, lds, a); rdA_all<S, I + 1>(f, lds, a); } }
template <int S, int I> DI void rdB_all(FragB& f, LDSP char* lds, const AddrB& a) { if constexpr (I < 16) { rdB<S, I>(f, lds, a); rdB_all<S, I + 1>(f, lds, a); } }

DI unsigned opaque(unsigned v) { asm volatile("" : "+v"(v)); return v; }
DI float row_sum16(float v) {
    v += __builtin_bit_cast(float, __builtin_amdgcn_update_dpp(0, __builtin_bit_cast(int, v), 0x128, 0xf, 0xf, false));
    v += __builtin_bit_cast(float, __builtin_amdgcn_update_dpp(0, __builtin_bit_cast(int, v), 0x124, 0xf, 0xf, false));
    v += __builtin_bit_cast(float, __builtin_amdgcn_update_dpp(0, __builtin_bit_cast(int, v), 0x122, 0xf, 0xf, false));
    v += __builtin_bit_cast(float, __builtin_amdgcn_update_dpp(0, __builtin_bit_cast(int, v), 0x121, 0xf, 0xf, false));
    return v;
}
DI float xsum16(float m) { u32x2 r = __builtin_amdgcn_permlane16_swap(__float_as_uint(m), __float_as_uint(m), false, false); return __uint_as_float(r[0]) + __uint_as_float(r[1]); }
DI float xsum32(float m) { u32x2 r = __builtin_amdgcn_permlane32_swap(__float_as_uint(m), __float_as_uint(m), false, false); return __uint_as_float(r[0]) + __uint_as_float(r[1]); }

template <bool X> DI void stage1(const Params& P, S1Ctx& cx, S2Regs& g2, int p, int cg, int p2, int pslot, int cslot, unsigned long long& ep_prod, unsigned long long& ep_cons) {
    LDSP char* lds = cx.lds;
    const int tid = threadIdx.x, l = tid & 63;
    const int w = __builtin_amdgcn_readfirstlane(tid >> 6);
    const int r = w >> 2, j = w & 3;
    const int a = l & 15, q = l >> 4;
    const int row = l >> 3, cp = l & 7;
    cx.xsrd = make_srd(P.x, SEQ * DM * 2);
    {
        const int tok = 128 * p + 32 * (w >> 1) + 2 * row + (w & 1);
        cx.xvo = tok * 1024 + ((cp ^ row) << 4);
        cx.xdst = LDS_X + w * 2048;
        FENCE();
        s1_op<X, 0, 0>(cx); s1_op<X, 0, 1>(cx);
        FENCE();
        if constexpr (X) {
            const int ah = 2 * w + (l >> 5), jd = l & 31;
            int ht = 128 * p - 16 + (ah < 8 ? 2 * ah : 2 * (ah - 8) + 1);
            ht = ht < 0 ? 0 : ht;
            cx.hvo = ht * 1024 + (((jd >> 2) ^ (ah & 7)) << 4) + (jd & 3) * 4;
            cx.hdst = LDS_X + 16384 + w * 256;
        }
    }
    {
        const int f = ((row >> 1) & 1) | ((j & 1) << 1);
        const unsigned colb = (unsigned)((((cp >> 1) ^ f) << 5) + ((cp & 1) << 4));
        const unsigned vo3072 = row * 3072 + colb, vo3584 = row * 3584 + colb;
        const i32x4 sq = make_srd(P.wqkv, DM * 3 * DM * 2), sg = make_srd(P.wg, DM * FF * 2), su = make_srd(P.wu, DM * FF * 2);
        if constexpr (X) {
            cx.srd[0] = sq; cx.wvo[0] = vo3072; cx.wso[0] = 8 * j * 3072 + r * 1024 + cg * 128; cx.wstr[0] = 32 * 3072; cx.wdst[0] = r * 4096 + j * 1024;
            cx.srd[1] = r ? sg : sq; cx.wvo[1] = r ? vo3584 : vo3072;
            cx.wso[1] = r ? 8 * j * 3584 + 2560 + cg * 128 : 8 * j * 3072 + 2048 + cg * 128;
            cx.wstr[1] = r ? 32 * 3584 : 32 * 3072; cx.wdst[1] = (2 + r) * 4096 + j * 1024;
            const int f2 = ((row >> 1) & 1) | (w & 2);
            cx.srd[2] = su; cx.wvo[2] = (row & 3) * 3584 + ((((cp >> 1) ^ f2) << 5) + ((cp & 1) << 4));
            cx.wso[2] = 4 * w * 3584 + 2560 + cg * 128; cx.wstr[2] = 32 * 3584; cx.wdst[2] = 16384 + w * 512;
        } else {
            const int fullb = cg * 320 + ((cg & 1) ? 64 : 0), halfb = cg * 320 + ((cg & 1) ? 0 : 256);
            cx.srd[0] = sg; cx.wvo[0] = vo3584; cx.wso[0] = 8 * j * 3584 + fullb + r * 128; cx.wstr[0] = 32 * 3584; cx.wdst[0] = r * 4096 + j * 1024;
            cx.srd[1] = su; cx.wvo[1] = vo3584; cx.wso[1] = cx.wso[0]; cx.wstr[1] = 32 * 3584; cx.wdst[1] = (2 + r) * 4096 + j * 1024;
            const int hr = l >> 2, hc = l & 3;
            cx.srd[2] = r ? su : sg;
            cx.wvo[2] = (hr & 7) * 3584 + ((((hc >> 1) ^ (w & 1)) << 5) + ((hc & 1) << 4));
            cx.wso[2] = 8 * j * 3584 + halfb; cx.wstr[2] = 32 * 3584; cx.wdst[2] = 16384 + w * 512;
        }
    }
    u32x4 gx, gw; u32x2 seed;
    s1_ops_all<X, 0, 2>(cx);
    FENCE();
    int lg = l, wg_ = w;
    asm volatile("" : "+v"(lg), "+s"(wg_));
    const int gtok = 128 * p + 8 * (2 * cg + (X ? 0 : 1)) + wg_;
    char* sync = P.ws + WS_SYNC;
    const char* seedp = sync + (lg < 32 ? p : p2) * SYNC_STRIDE + ((lg & 31) == 1 ? 128 : 0);
    if (lg == 2) seedp = P.ws + WS_G + (gtok & ~1) * 4;
    asm volatile("global_load_dwordx2 %2, %5, off sc1\n\tglobal_load_dwordx4 %0, %3, off\n\tglobal_load_dwordx4 %1, %4, off"
                 : "=&v"(gx), "=&v"(gw), "=&v"(seed)
                 : "v"(P.x + gtok * 1024 + lg * 16), "v"(P.gw + lg * 16), "v"(seedp) : "memory");
    barrier();
    s1_ops_all<X, 1, 0>(cx);
    FENCE();
    {
        int lo = l, wo_ = w;
        asm volatile("" : "+v"(lo), "+s"(wo_));
        const int mh = wo_ & 1, kq = wo_ >> 1, row = lo >> 3, cp = lo & 7;
        cx.wosrd = make_srd(P.wo, DM * DM * 2);
        cx.wdsrd = make_srd(P.wd, FF * DM * 2);
        const int hf = (cp >> 2) ^ ((row >> 1) & 1);
        cx.w2vo = (unsigned)((8 * mh + row) * 1024 + cx.nb * 128 + hf * 64 + (cp & 3) * 16);
        cx.w2dst = (unsigned)(kq * 2048 + mh * 1024);
        const int gw_ = blockIdx.x * 8 + wo_;
        cx.touch = lo < 2 ? P.wo + (2 * gw_ + lo) * 128 : P.wd + (7 * gw_ + (lo < 9 ? lo - 2 : 6)) * 128;
        { const int t = lo < 12 ? lo >> 1 : 5; cx.touch2 = P.wo + (size_t)((8 * kq + 2 + t) * 16 + 8 * mh + 2 * (blockIdx.x >> 6) + (lo & 1)) * 1024 + cx.nb * 128; }
        cx.codevo = (unsigned)((((blockIdx.x >> 3) * 8 + wo_) * 2 + (lo & 1)) * 128);
        const int wrow = 8 * (lo >> 5) + ((lo & 15) >> 2);
        cx.wl = (unsigned)(wrow * 128 + (((wrow >> 1) & 1) << 6) + ((lo >> 4) & 1) * 32 + (lo & 3) * 8);
    }
    asm volatile("s_waitcnt vmcnt(%3)" : "+v"(gx), "+v"(gw), "+v"(seed) : "n"(SchedOf<X>::v.n[1]) : "memory");
    {
        const unsigned s0 = seed[0], s1 = seed[1];
        ep_prod = (((unsigned long long)(unsigned)__builtin_amdgcn_readlane(s1, 0) << 32) | (unsigned)__builtin_amdgcn_readlane(s0, 0)) + 1ull;
        ep_cons = (((unsigned long long)(unsigned)__builtin_amdgcn_readlane(s1, 32) << 32) | (unsigned)__builtin_amdgcn_readlane(s0, 32)) + 1ull;
        if (tid == 0) {
            __hip_atomic_store((GLBP unsigned long long*)(sync + p2 * SYNC_STRIDE + 256 + cslot * 8), ep_cons, __ATOMIC_RELAXED, __HIP_MEMORY_SCOPE_AGENT);
            __hip_atomic_store((GLBP unsigned long long*)(sync + p * SYNC_STRIDE + 384 + pslot * 8), ep_prod, __ATOMIC_RELAXED, __HIP_MEMORY_SCOPE_AGENT);
        }
    }
    float gl = 0.f;
    for (int i = 0; i < 4; ++i) {
        const unsigned xa_ = gx[i], wa_ = gw[i];
        gl = __builtin_amdgcn_fdot2_f32_bf16(__builtin_bit_cast(bf16x2, xa_), __builtin_bit_cast(bf16x2, wa_), gl, false);
    }
    const float gate = sigmoidf_(xsum32(xsum16(row_sum16(gl))));

    const int qp = a >> 2, pp = a & 3;
    const int fsw = ((qp >> 1) & 1) | ((q & 1) << 1);
    const unsigned xa0 = LDS_X + (64 * r + a) * 128 + ((q ^ (a & 7)) << 4);
    char* act = P.ws + WS_ACT;
    const int rb = 2 * p + r;
    wait_vm<in_flight_ok(X, 0, 2)>();
    barrier();
    if constexpr (X) {
        AddrA ad;
        ad.w[0] = (8 * q + qp) * 128 + ((j ^ fsw) << 5) + pp * 8; ad.w[1] = opaque(ad.w[0] + 2 * WSLAB);
        ad.x[0] = xa0; ad.x[1] = xa0 ^ 64u;
        const int hrow = r == 0 ? 128 + a : 40 + a + (a >= 8 ? 8 : 0);
        ad.h[0] = LDS_X + hrow * 128 + ((q ^ (a & 7)) << 4); ad.h[1] = ad.h[0] ^ 64u;
        const float NINF = -__builtin_inff();
        f32x4 bc, bp, bh0, bh1;
        const bool first = (p == 0) && (r == 0);
        for (int i = 0; i < 4; ++i) {
            const int b = 4 * q + i;
            bc[i] = (a - b >= 0 && a - b <= 7) ? 0.f : NINF;
            bp[i] = (b >= a + 9) ? 0.f : NINF;
            bh0[i] = (!first && b >= a + 1 && b <= 7) ? 0.f : NINF;
            bh1[i] = first ? NINF : bp[i];
        }
        asm volatile("" : "+v"(bc), "+v"(bp), "+v"(bh0), "+v"(bh1));
        AccA acc;
        for (int i = 0; i < 4; ++i) { acc.q[i] = 0.f; acc.g[i] = 0.f; acc.u[i] = 0.f; }
        for (int i = 0; i < 5; ++i) { acc.k[i] = 0.f; acc.v[i] = 0.f; }
        FragA f0, f1;
        rdA_all<0, 0>(f0, lds, ad);
        stepA<0>(cx, g2, acc, f0, f1, ad);
        wait_lgkm0();
        barrier();
        FENCE();
        {
            const int T = 32 + 80 + 4 * cg + j;
            u32x2 h[4];
            if (r) __builtin_amdgcn_s_setprio(1);
            for (int i = 0; i < 4; ++i) glu_scale(acc.g[i]);
            issue_l<0>(cx);
            for (int i = 0; i < 4; ++i) glu_exp(acc.g[i]);
            issue_l<1>(cx);
            for (int i = 0; i < 4; ++i) glu_add(acc.g[i]);
            issue_l<2>(cx);
            for (int i = 0; i < 4; ++i) glu_rcp(acc.g[i]);
            issue_l<3>(cx);
            for (int i = 0; i < 4; ++i) h[i] = pk4(acc.u[i] * acc.g[i]);
            issue_l<4>(cx);
            for (int tg = 0; tg < 2; ++tg)
                st16(act, (unsigned)(((rb * NKT + T) * 2 + tg) * 1024 + l * 16), frag32(h[2 * tg], h[2 * tg + 1]));
        }
        issue_l<5>(cx);
        const float c2 = 0.25f * 1.4426950408889634f;
        u32x2 qb[4], kb[5], vb[5], ob[4];
        for (int i = 0; i < 4; ++i) qb[i] = pk4(acc.q[i]);
        issue_l<6>(cx);
        for (int i = 0; i < 5; ++i) kb[i] = pk4(acc.k[i]);
        issue_l<7>(cx);
        f32x4 sp[4], sc[4];
        for (int R = 0; R < 4; ++R) {
            sp[R] = mfma16(kb[R >= 2 ? R - 2 : 4], qb[R], R == 0 ? bh0 : R == 1 ? bh1 : bp);
            sc[R] = mfma16(kb[R], qb[R], bc);
        }
        issue_l<8>(cx);
        for (int i = 0; i < 5; ++i) vb[i] = pk4(acc.v[i]);
        issue_l<9>(cx);
        __builtin_amdgcn_s_setprio(0);
        float m[4];
        for (int R = 0; R < 4; ++R) m[R] = max8(sp[R], sc[R]);
        colmax4(m[0], m[1], m[2], m[3]);
        for (int R = 0; R < 4; ++R) m[R] *= -c2;
        FENCE();
        const u32x4 onesu = {0x3f803f80u, 0x3f803f80u, 0x3f803f80u, 0x3f803f80u};
        const bf16x8 ones = __builtin_bit_cast(bf16x8, onesu);
        const f32x4 z4 = {0.f, 0.f, 0.f, 0.f};
        f32x4 o[4], sm[4];
        for (int R = 0; R < 4; ++R) {
            for (int i = 0; i < 4; ++i) {
                sp[R][i] = __builtin_amdgcn_exp2f(__builtin_fmaf(sp[R][i], c2, m[R]));
                sc[R][i] = __builtin_amdgcn_exp2f(__builtin_fmaf(sc[R][i], c2, m[R]));
            }
            const bf16x8 pf = cat8(pk4(sp[R]), pk4(sc[R]));
            o[R] = mfma32(cat8(vb[R >= 2 ? R - 2 : 4], vb[R]), pf, z4);
            sm[R] = mfma32(ones, pf, z4);
            if (R == 0) issue_l<10>(cx); else if (R == 1) issue_l<11>(cx); else if (R == 2) issue_l<12>(cx); else issue_l<13>(cx);
        }
        for (int R = 0; R < 4; ++R) ob[R] = pk4(o[R] * __builtin_amdgcn_rcpf(sm[R][0]));
        const int T = 4 * cg + j;
        for (int tg = 0; tg < 2; ++tg)
            st16(act, (unsigned)(((rb * NKT + T) * 2 + tg) * 1024 + l * 16), frag32(ob[2 * tg], ob[2 * tg + 1]));
    } else {
        AddrB ad;
        ad.w[0][0] = (j >> 1) * 4096 + (8 * q + qp) * 128 + (((2 * (j & 1)) ^ fsw) << 5) + pp * 8;
        ad.w[0][1] = ad.w[0][0] ^ 32u;
        ad.wh[0] = 16384 + (8 * q + qp) * 64 + (((j >> 1) ^ (q & 1)) << 5) + pp * 8;
        ad.w[1][0] = opaque(ad.w[0][0] + 2 * WSLAB); ad.w[1][1] = opaque(ad.w[0][1] + 2 * WSLAB); ad.wh[1] = opaque(ad.wh[0] + 2 * WSLAB);
        const unsigned rot = 4096u * (j & 1);
        ad.x[0] = xa0 + rot; ad.x[1] = (xa0 ^ 64u) + rot; ad.x[2] = xa0 - rot; ad.x[3] = (xa0 ^ 64u) - rot;
        AccB acc;
        for (int i = 0; i < 4; ++i) { acc.g0[i] = 0.f; acc.u0[i] = 0.f; acc.g1[i] = 0.f; acc.u1[i] = 0.f; }
        for (int i = 0; i < 2; ++i) { acc.g2[i] = 0.f; acc.u2[i] = 0.f; }
        FragB f0, f1;
        rdB_all<0, 0>(f0, lds, ad);
        stepB<0>(cx, g2, acc, f0, f1, ad);
        wait_lgkm0();
        barrier();
        FENCE();
        const int fbase = 32 + 10 * cg;
        const int T0 = fbase + ((cg & 1) ? 2 : 0) + 2 * j, T2 = fbase + ((cg & 1) ? 0 : 8) + (j >> 1);
        u32x2 h0[4], h1[4], h2[2];
        const int tg0 = j & 1;
        if (r) __builtin_amdgcn_s_setprio(1);
        for (int i = 0; i < 2; ++i) { glu_scale(acc.g0[i]); glu_scale(acc.g1[i]); glu_scale(acc.g2[i]); }
        issue_l<0>(cx);
        for (int i = 0; i < 2; ++i) { glu_exp(acc.g0[i]); glu_exp(acc.g1[i]); glu_exp(acc.g2[i]); }
        issue_l<1>(cx);
        for (int i = 0; i < 2; ++i) { glu_add(acc.g0[i]); glu_add(acc.g1[i]); glu_add(acc.g2[i]); }
        issue_l<2>(cx);
        for (int i = 0; i < 2; ++i) { glu_rcp(acc.g0[i]); glu_rcp(acc.g1[i]); glu_rcp(acc.g2[i]); }
        issue_l<3>(cx);
        for (int i = 0; i < 2; ++i) { h0[i] = pk4(acc.u0[i] * acc.g0[i]); h1[i] = pk4(acc.u1[i] * acc.g1[i]); h2[i] = pk4(acc.u2[i] * acc.g2[i]); }
        issue_l<4>(cx);
        st16(act, (unsigned)(((rb * NKT + T0) * 2 + tg0) * 1024 + l * 16), frag32(h0[0], h0[1]));
        st16(act, (unsigned)(((rb * NKT + T0 + 1) * 2 + tg0) * 1024 + l * 16), frag32(h1[0], h1[1]));
        st16(act, (unsigned)(((rb * NKT + T2) * 2 + tg0) * 1024 + l * 16), frag32(h2[0], h2[1]));
        issue_l<5>(cx);
        __builtin_amdgcn_s_setprio(0);
        for (int i = 2; i < 4; ++i) { glu_scale(acc.g0[i]); glu_scale(acc.g1[i]); }
        issue_l<6>(cx); issue_l<7>(cx);
        for (int i = 2; i < 4; ++i) { glu_exp(acc.g0[i]); glu_exp(acc.g1[i]); }
        issue_l<8>(cx); issue_l<9>(cx);
        for (int i = 2; i < 4; ++i) { glu_add(acc.g0[i]); glu_add(acc.g1[i]); }
        issue_l<10>(cx); issue_l<11>(cx);
        for (int i = 2; i < 4; ++i) { glu_rcp(acc.g0[i]); glu_rcp(acc.g1[i]); }
        issue_l<12>(cx); issue_l<13>(cx);
        for (int i = 2; i < 4; ++i) { h0[i] = pk4(acc.u0[i] * acc.g0[i]); h1[i] = pk4(acc.u1[i] * acc.g1[i]); }
        st16(act, (unsigned)(((rb * NKT + T0) * 2 + (tg0 ^ 1)) * 1024 + l * 16), frag32(h0[2], h0[3]));
        st16(act, (unsigned)(((rb * NKT + T0 + 1) * 2 + (tg0 ^ 1)) * 1024 + l * 16), frag32(h1[2], h1[3]));
    }
    if (l == 0) __hip_atomic_store((GLBP float*)(P.ws + WS_G) + gtok, gate, __ATOMIC_RELAXED, __HIP_MEMORY_SCOPE_AGENT);
    wait_vm<NL1 + 1 + (X ? 4 : 5)>();
}

DI f32x16 mfma3232(bf16x8 a, bf16x8 b, f32x16 c) { return __builtin_amdgcn_mfma_f32_32x32x16_bf16(a, b, c, 0, 0, 0); }
template <int I> DI void s2_load_a(const S1Ctx& c, S2Regs& g, __amdgpu_buffer_rsrc_t ar, unsigned avo) {
    if constexpr (I < NR + NL) {
        const u32x4 v = __builtin_amdgcn_raw_buffer_load_b128(ar, avo, s2_T(c, I) * 2048u, AUX_SC1);
        g.a[I % ADEPTH] = __builtin_bit_cast(bf16x8, v);
    }
}
template <int I> DI void s2_read_w(const S1Ctx& c, bf16x8 (&w)[2]) {
    const unsigned cell = l_cell(I, c.kq);
    w[0] = lds_tr(c.lds, c.wl + cell, 512); w[1] = lds_tr(c.lds, (c.wl ^ 64u) + cell, 512);
}
typedef const GLBP unsigned long long* flagp;
DI unsigned long long ld_flag(flagp p) { return __hip_atomic_load(p, __ATOMIC_RELAXED, __HIP_MEMORY_SCOPE_AGENT); }
DI void spin_until(flagp p, unsigned long long want) {
    for (unsigned spins = 0; spins < (1u << 18); ++spins) {
        if (__all(ld_flag(p) == want)) break;
        __builtin_amdgcn_s_sleep(1);
    }
}
struct S2Sync { flagp xflags, gflags; unsigned long long xseen, gseen, want; GLBP unsigned long long* seed; bool bump; const GLBP float* gatep; float gate; };
constexpr int XCHECK = 20 - ADEPTH, XLOOK = XCHECK - 5, GLOOK = 16, GCHECK = 24;
template <int I> DI void s2_tile(const S1Ctx& c, S2Regs& g, S2Sync& sy, bf16x8 (&wl)[3][2], __amdgpu_buffer_rsrc_t ar, unsigned avo) {
    if constexpr (I < NR + NL) {
        if constexpr (I == XCHECK) {
            if (!__all(sy.xseen == sy.want)) spin_until(sy.xflags, sy.want);
            if (sy.bump) __hip_atomic_store(sy.seed, sy.want, __ATOMIC_RELAXED, __HIP_MEMORY_SCOPE_AGENT);
            FENCE();
        }
        if constexpr (I == GCHECK) {
            if (!__all(sy.gseen == sy.want)) spin_until(sy.gflags, sy.want);
            sy.gate = __hip_atomic_load(sy.gatep, __ATOMIC_RELAXED, __HIP_MEMORY_SCOPE_AGENT);
            FENCE();
        }
        if constexpr (I + 2 >= NR && I + 2 < NR + NL) s2_read_w<I + 2>(c, wl[(I + 2) % 3]);
        const bf16x8 w0 = I < NR ? g.wr[I < NR ? I : 0][0] : wl[I % 3][0], w1 = I < NR ? g.wr[I < NR ? I : 0][1] : wl[I % 3][1];
        const bf16x8 av = g.a[I % ADEPTH];
        if constexpr (I >= 28) { g.accA[0] = mfma3232(w0, av, g.accA[0]); g.accA[1] = mfma3232(w1, av, g.accA[1]); }
        else { g.accB[0] = mfma3232(w0, av, g.accB[0]); g.accB[1] = mfma3232(w1, av, g.accB[1]); }
        FENCE();
        s2_load_a<I + ADEPTH>(c, g, ar, avo);
        if constexpr (I == XLOOK) sy.xseen = ld_flag(sy.xflags);
        if constexpr (I == GLOOK) sy.gseen = ld_flag(sy.gflags);
        FENCE();
        s2_tile<I + 1>(c, g, sy, wl, ar, avo);
    }
}
template <int I, int E> DI void s2_prefetch_a(const S1Ctx& c, S2Regs& g, __amdgpu_buffer_rsrc_t ar, unsigned avo) {
    if constexpr (I < E) { s2_load_a<I>(c, g, ar, avo); s2_prefetch_a<I + 1, E>(c, g, ar, avo); }
}

DI void stage2(const Params& P, S1Ctx& c, S2Regs& g, int p, int pslot, int rb, int nb, int cslot, unsigned long long ep_prod, unsigned long long ep_cons) {
    LDSP char* lds = c.lds;
    const int tid = threadIdx.x, l = tid & 63;
    const int w = __builtin_amdgcn_readfirstlane(tid >> 6);
    const int mh = w & 1, kq = w >> 1;
    const int h = l >> 5;
    char* sync = P.ws + WS_SYNC;
    const int p2 = rb >> 1;
    c.wosrd = make_srd_s(P.wo, DM * DM * 2);
    const bool bumper = cslot == 0 && w == 0;
    const flagp yflags = (flagp)(sync + p2 * SYNC_STRIDE + 128 + (8 + 2 * kq + (l & 1)) * 8);
    S2Sync sy;
    sy.xflags = (flagp)(sync + p2 * SYNC_STRIDE + ((bumper && l >= 32) ? 256 + (l - 32) * 8 : 128 + (2 * kq + (l & 1)) * 8));
    { const int gp = 8 * (rb & 1) + (l & 7); sy.gflags = (flagp)(sync + p2 * SYNC_STRIDE + 128 + ((gp & 1) * 8 + (gp >> 1)) * 8); }
    const int pos = l & 31;
    sy.gatep = (const GLBP float*)(P.ws + WS_G) + 64 * rb + 32 * mh + (pos < 16 ? 2 * pos : 2 * (pos - 16) + 1);
    sy.want = ep_cons; sy.seed = (GLBP unsigned long long*)(sync + p2 * SYNC_STRIDE); sy.bump = bumper && l == 0;

    barrier();
#pragma unroll
    for (int i = NR1; i < NR; ++i) {
        g.wr[i][0] = lds_tr(lds, c.wl + kq * 2048 + r_cell(i), 512);
        g.wr[i][1] = lds_tr(lds, (c.wl ^ 64u) + kq * 2048 + r_cell(i), 512);
    }
    wait_vm<0>();
    wait_lgkm0();
    barrier();
    if (tid == 0) __hip_atomic_store((GLBP unsigned long long*)(sync + p * SYNC_STRIDE + 128 + pslot * 8), ep_prod, __ATOMIC_RELAXED, __HIP_MEMORY_SCOPE_AGENT);
    unsigned long long yseen;
#pragma unroll
    for (int i = NR + NL1; i < NR + NL; ++i) dma16b_rf(c.wosrd, c.w2vo, s2_T(c, i) * 16384u, l_cell(i, kq) + mh * 1024u);
    asm volatile("global_load_dwordx2 %0, %1, off sc1" : "=&v"(yseen) : "v"(yflags) : "memory");
    asm volatile("s_waitcnt vmcnt(0)" : "+v"(yseen) :: "memory");
    barrier();
    if (!__all(yseen == ep_cons)) spin_until(yflags, ep_cons);

    const __amdgpu_buffer_rsrc_t ar = __builtin_amdgcn_make_buffer_rsrc((void*)(P.ws + WS_ACT + (size_t)rb * NKT * 2048), 0, NKT * 2048, 0x00020000);
    const unsigned avo = (unsigned)(mh * 1024 + l * 16);
    FENCE();
    s2_prefetch_a<0, ADEPTH>(c, g, ar, avo);
    for (int i = 0; i < 16; ++i) { g.accA[0][i] = 0.f; g.accA[1][i] = 0.f; g.accB[0][i] = 0.f; g.accB[1][i] = 0.f; }
    bf16x8 wl[3][2];
    FENCE();
    s2_tile<0>(c, g, sy, wl, ar, avo);
    const float gate = sy.gate;

    {
        const float gm = 1.f - gate;
#pragma unroll
        for (int nt = 0; nt < 2; ++nt)
#pragma unroll
            for (int i = 0; i < 16; ++i) g.accB[nt][i] = gate * g.accA[nt][i] + gm * g.accB[nt][i];
    }
    FENCE();
    wait_lgkm0();
    barrier();
    {
        const int row = mh * 32 + pos;
#pragma unroll
        for (int nt = 0; nt < 2; ++nt)
#pragma unroll
            for (int gp = 0; gp < 2; ++gp) {
                u32x2 lo = {pk2(g.accB[nt][8 * gp], g.accB[nt][8 * gp + 1]), pk2(g.accB[nt][8 * gp + 2], g.accB[nt][8 * gp + 3])};
                u32x2 hi = {pk2(g.accB[nt][8 * gp + 4], g.accB[nt][8 * gp + 5]), pk2(g.accB[nt][8 * gp + 6], g.accB[nt][8 * gp + 7])};
                const u32x2 s0 = __builtin_amdgcn_permlane32_swap(lo[0], hi[0], false, false);
                const u32x2 s1 = __builtin_amdgcn_permlane32_swap(lo[1], hi[1], false, false);
                const u32x4 v = {s0[0], s1[0], s0[1], s1[1]};
                const int c8 = nt * 4 + 2 * gp + h;
                *(LDSP u32x4*)(lds + kq * 8192 + row * 128 + ((c8 ^ (row & 7)) << 4)) = v;
            }
    }
    wait_lgkm0();
    barrier();
    {
        const int row = tid >> 3, c8 = tid & 7;
        f32x4 s0 = {0.f, 0.f, 0.f, 0.f}, s1 = s0;
#pragma unroll
        for (int k = 0; k < 4; ++k) {
            const u32x4 v = *(LDSP u32x4*)(lds + k * 8192 + row * 128 + ((c8 ^ (row & 7)) << 4));
            s0[0] += __uint_as_float(v[0] << 16); s0[1] += __uint_as_float(v[0] & 0xffff0000u);
            s0[2] += __uint_as_float(v[1] << 16); s0[3] += __uint_as_float(v[1] & 0xffff0000u);
            s1[0] += __uint_as_float(v[2] << 16); s1[1] += __uint_as_float(v[2] & 0xffff0000u);
            s1[2] += __uint_as_float(v[3] << 16); s1[3] += __uint_as_float(v[3] & 0xffff0000u);
        }
        const int ps = row & 31;
        const int tok = 64 * rb + (row & 32) + (ps < 16 ? 2 * ps : 2 * (ps - 16) + 1);
        const u32x2 a = pk4(s0), b = pk4(s1);
        const u32x4 o = {a[0], a[1], b[0], b[1]};
        st16(P.out, (unsigned)(tok * 1024 + nb * 128 + c8 * 16), o);
    }
}

__global__ __launch_bounds__(512, 2) void k_fused(Params P) {
    extern __shared__ __attribute__((aligned(16))) char smem[];
    const int b = blockIdx.x, xl = b & 7, idx = b >> 3;
    const int p = 8 * (xl & 1) + (idx & 7), gi = idx >> 3, cg = 2 * (xl >> 1) + (gi & 1);
    const int rb = 4 * xl + (idx >> 3), nb = idx & 7;
    const int pslot = (gi < 2 ? 0 : 8) + cg, cslot = (rb & 1) * 8 + nb;
    S1Ctx cx; cx.lds = (LDSP char*)smem;
    { unsigned long long pc; asm volatile("s_getpc_b64 %0" : "=s"(pc)); cx.code = (const char*)(pc & ~127ull); }
    cx.rot = (unsigned)((idx & 3) * 2 + (xl & 1));
    { const int w_ = __builtin_amdgcn_readfirstlane(threadIdx.x >> 6); cx.kq = w_ >> 1; cx.mh = w_ & 1; }
    cx.nb = nb;
    unsigned long long ep_prod, ep_cons;
    S2Regs g2;
    if (gi < 2) stage1<true>(P, cx, g2, p, cg, rb >> 1, pslot, cslot, ep_prod, ep_cons);
    else stage1<false>(P, cx, g2, p, cg, rb >> 1, pslot, cslot, ep_prod, ep_cons);
    stage2(P, cx, g2, p, pslot, rb, nb, cslot, ep_prod, ep_cons);
}

}

extern "C" void kernel_launch(void* const* d_in, const int* in_sizes, int n_in, void* d_out, int out_size, void* d_ws, size_t ws_size,
                              hipStream_t stream) {
    Params P;
    P.x = (const char*)d_in[0]; P.wqkv = (const char*)d_in[1]; P.wo = (const char*)d_in[2];
    P.wg = (const char*)d_in[4]; P.wu = (const char*)d_in[5]; P.wd = (const char*)d_in[6]; P.gw = (const char*)d_in[7];
    P.out = (char*)d_out; P.ws = (char*)d_ws;
    (void)hipFuncSetAttribute((const void*)k_fused, hipFuncAttributeMaxDynamicSharedMemorySize, LDS_TOTAL);
    k_fused<<<256, 512, LDS_TOTAL, stream>>>(P);
}
```
